```python
import torch

def _softmax(x, axis=-1):
    return torch.softmax(x, dim=axis)
def _gelu(x):
    return torch.nn.functional.gelu(x, approximate="tanh")
def _silu(x):
    return torch.nn.functional.silu(x)
def _sigmoid(x):
    return torch.sigmoid(x)
def _layernorm(x, axis=-1, eps=1e-5):
    m = x.mean(dim=axis, keepdim=True)
    v = x.var(dim=axis, keepdim=True, unbiased=False)
    return (x - m) / torch.sqrt(v + eps)
def _scatter_add(tgt, idx, src, axis=0):
    return tgt.clone().index_add_(axis, idx, src)
def _segment_sum(src, seg, n):
    out = torch.zeros((n,) + src.shape[1:], dtype=src.dtype)
    return out.index_add_(0, seg, src)
def _pad(x, pw):
    flat = tuple(v for pair in reversed(pw) for v in pair)
    return torch.nn.functional.pad(x, flat)

kernel_name = 'parallel_sum__compressed_kv_cla__dense_activation_squared_relu__prompt'

def setup_inputs():

    _w = (lambda a: (lambda t: t.to(torch.bfloat16) if t.is_floating_point() else t)(torch.as_tensor(a)))
    def _A_setup():
        B, H, Sq, Sk, D, M = 1, 32, 2048, 2048, 128, 512
        Dc = 4 * D
        return {
            "x": _w((lambda *s: torch.randn(*s, dtype=torch.bfloat16) * 0.05)(B, Sq, M)),
            "c_kv": _w((lambda *s: torch.randn(*s, dtype=torch.bfloat16) * 1.6)(B, Sk, Dc)),
            "w_q": _w((lambda *s: torch.randn(*s, dtype=torch.bfloat16) * 1.6)(M, H, D) * (M ** -0.5)),
            "w_uk": _w((lambda *s: torch.randn(*s, dtype=torch.bfloat16) * 1.6)(Dc, H, D) * (Dc ** -0.5)),
            "w_uv": _w((lambda *s: torch.randn(*s, dtype=torch.bfloat16) * 1.6)(Dc, H, D) * (Dc ** -0.5)),
            "w_o": _w((lambda *s: torch.randn(*s, dtype=torch.bfloat16) * 1.6)(H, D, M) * ((H * D) ** -0.5)),
        }
    _A_in = _A_setup()

    def _B_setup():
        B, S, D, F = 1, 2048, 512, 1792
        return {
            "x": _w((lambda *s: torch.randn(*s, dtype=torch.bfloat16) * 0.05)(B, S, D)),
            "w1": _w((lambda *s: torch.randn(*s, dtype=torch.bfloat16) * 0.05)(D, F)),
            "b1": _w((lambda *s: torch.randn(*s, dtype=torch.bfloat16) * 0.4)(F)),
            "w2": _w((lambda *s: torch.randn(*s, dtype=torch.bfloat16) * 0.05)(F, D)),
            "b2": _w((lambda *s: torch.randn(*s, dtype=torch.bfloat16) * 1.6)(D)),
        }
    _B_in = _B_setup()

    return {
        "A_x": _A_in["x"],
        "A_c_kv": _A_in["c_kv"],
        "A_w_q": _A_in["w_q"],
        "A_w_uk": _A_in["w_uk"],
        "A_w_uv": _A_in["w_uv"],
        "A_w_o": _A_in["w_o"],
        "B_x": _B_in["x"],
        "B_w1": _B_in["w1"],
        "B_b1": _B_in["b1"],
        "B_w2": _B_in["w2"],
        "B_b2": _B_in["b2"],
    }

def reference(A_x, A_c_kv, A_w_q, A_w_uk, A_w_uv, A_w_o, B_x, B_w1, B_b1, B_w2, B_b2):
    def _A_ref(x, c_kv, w_q, w_uk, w_uv, w_o):
        B, Sq, M = x.shape[0], x.shape[1], x.shape[2]
        Sk = c_kv.shape[1]
        H, D = w_q.shape[1], w_q.shape[2]
        q = torch.einsum("bqm,mhd->bhqd", x, w_q)
        k = torch.einsum("bkc,chd->bhkd", c_kv, w_uk)
        v = torch.einsum("bkc,chd->bhkd", c_kv, w_uv)
        scores = torch.einsum("bhqd,bhkd->bhqk", q, k) * (D ** -0.5)
        off = Sk - Sq
        qi = torch.arange(Sq)[:, None]
        kj = torch.arange(Sk)[None, :]
        cmask = kj <= (qi + off)
        scores = torch.where(cmask[None, None], scores, -1e9)
        p = _softmax(scores, axis=-1)
        ctx = torch.einsum("bhqk,bhkd->bhqd", p, v)
        return torch.einsum("bhqd,hdm->bqm", ctx, w_o)

    def _B_ref(x, w1, b1, w2, b2):
        h = torch.einsum("bsd,df->bsf", x, w1) + b1
        zero = torch.zeros_like(h)
        r = torch.where(h > zero, h, zero)
        h = r * r
        out = torch.einsum("bsf,fd->bsd", h, w2) + b2
        return out

    x = A_x
    xn = _layernorm(x, axis=-1)
    return x + _A_ref(xn, A_c_kv, A_w_q, A_w_uk, A_w_uv, A_w_o) + _B_ref(xn, B_w1, B_b1, B_w2, B_b2)
```

```cpp
#include <hip/hip_runtime.h>
#include <hip/hip_bf16.h>
#include <stdint.h>

typedef unsigned short u16;
typedef short bf16x8 __attribute__((ext_vector_type(8)));
typedef short s16x4 __attribute__((ext_vector_type(4)));
typedef short v4i16_t __attribute__((ext_vector_type(4)));
typedef float f32x2 __attribute__((ext_vector_type(2)));
typedef float f32x4 __attribute__((ext_vector_type(4)));
typedef float f32x16 __attribute__((ext_vector_type(16)));
typedef unsigned u32x4 __attribute__((ext_vector_type(4)));
typedef unsigned u32x2 __attribute__((ext_vector_type(2)));
typedef __bf16 bf16x2_t __attribute__((ext_vector_type(2)));
#define LAS __attribute__((address_space(3)))
#define DI __device__ __forceinline__

constexpr int SEQ = 2048, DM = 512, NH = 32, HD = 128, FF = 1792, KTOT = NH * HD + FF, NKT = KTOT / 64;
constexpr size_t MiB = 1024 * 1024;
constexpr size_t OFF_K = 0, OFF_V = 16 * MiB, OFF_XN = 32 * MiB, OFF_A = 34 * MiB, OFF_SLAB = 58 * MiB, OFF_CNT = 74 * MiB;

DI unsigned cvtpk(float lo, float hi) { f32x2 v = {lo, hi}; bf16x2_t b = __builtin_convertvector(v, bf16x2_t); return __builtin_bit_cast(unsigned, b); }
DI float rbf(float x) { return __uint_as_float(cvtpk(x, x) & 0xffff0000u); }
DI float bflo(unsigned w) { return __uint_as_float(w << 16); }
DI float bfhi(unsigned w) { return __uint_as_float(w & 0xffff0000u); }
DI s16x4 vtr(LAS const char* p) { return __builtin_bit_cast(s16x4, __builtin_amdgcn_ds_read_tr16_b64_v4i16((LAS v4i16_t*)p)); }
DI bf16x8 vtr2(LAS const char* p0, LAS const char* p1) { s16x4 a = vtr(p0), b = vtr(p1); return __builtin_shufflevector(a, b, 0, 1, 2, 3, 4, 5, 6, 7); }
DI int rfl(int x) { return __builtin_amdgcn_readfirstlane(x); }
#define SCHED __builtin_amdgcn_sched_barrier(0)
#define WAIT_V(n) asm volatile("s_waitcnt vmcnt(" #n ")" ::: "memory")
#define WAIT_L(n) asm volatile("s_waitcnt lgkmcnt(" #n ")" ::: "memory")
#define BAR __builtin_amdgcn_s_barrier()
#define MFMA32(a, b, c) __builtin_amdgcn_mfma_f32_32x32x16_bf16((a), (b), (c), 0, 0, 0)
#define MFMA16(a, b, c) __builtin_amdgcn_mfma_f32_16x16x32_bf16((a), (b), (c), 0, 0, 0)

DI void dma1k(const void* sbase, unsigned voff, unsigned ldsdst) {
  unsigned sv;
  asm volatile("s_mov_b32 %0, m0\n\ts_mov_b32 m0, %3\n\ts_nop 0\n\t"
               "global_load_lds_dwordx4 %1, %2\n\t"
               "s_mov_b32 m0, %0" : "=&s"(sv) : "v"(voff), "s"(sbase), "s"(ldsdst) : "memory");
}
DI void dma2k(const void* sbase, unsigned voff, unsigned ldsdst) {
  unsigned sv;
  asm volatile("s_mov_b32 %0, m0\n\ts_mov_b32 m0, %3\n\ts_nop 0\n\t"
               "global_load_lds_dwordx4 %1, %2\n\t"
               "global_load_lds_dwordx4 %1, %2 offset:1024\n\t"
               "s_mov_b32 m0, %0" : "=&s"(sv) : "v"(voff), "s"(sbase), "s"(ldsdst) : "memory");
}
DI void dma4k(const void* sbase, unsigned voff, unsigned ldsdst) {
  unsigned sv;
  asm volatile("s_mov_b32 %0, m0\n\ts_mov_b32 m0, %3\n\ts_nop 0\n\t"
               "global_load_lds_dwordx4 %1, %2\n\t"
               "global_load_lds_dwordx4 %1, %2 offset:1024\n\t"
               "global_load_lds_dwordx4 %1, %2 offset:2048\n\t"
               "global_load_lds_dwordx4 %1, %2 offset:3072\n\t"
               "s_mov_b32 m0, %0" : "=&s"(sv) : "v"(voff), "s"(sbase), "s"(ldsdst) : "memory");
}

constexpr int K1_HTB = 128 * 64 * 2, K1_LDS = 8 * K1_HTB;
DI int lds_byte(int r, int c) { const int st = (r >> 4) * 2 + (c >> 5), rr = r & 15, cc = c & 31, ob = rr * 64 + cc * 2; return st * 1024 + (ob ^ (((ob >> 9) & 1) << 5)); }
DI void stage_rc(int b, int& R, int& C) { const int st = b / 1024, sb = b % 1024, swz = sb ^ (((sb >> 9) & 1) << 5); R = (st >> 1) * 16 + swz / 64; C = (st & 1) * 32 + (swz % 64) / 2; }

template <bool ISV>
DI void k1_body(const u16* __restrict__ A, const u16* __restrict__ W, u16* __restrict__ out, int pm, int pn, LAS char* lds,
                const u16* __restrict__ xrow, u16* __restrict__ xn_t, int row) {
  const int tid = threadIdx.x, wid = rfl(tid >> 6), lane = tid & 63, wr = wid >> 2, wc = wid & 3, fr = lane & 15, fq = lane >> 4;
  u32x4 xv;
  asm volatile("global_load_dwordx4 %0, %1, off" : "=v"(xv) : "v"(xrow + lane * 8) : "memory");
  unsigned voffA[2], voffB[2];
#pragma unroll
  for (int i = 0; i < 2; ++i) {
    int R, C; stage_rc(tid * 16 + i * 8192, R, C); voffA[i] = (unsigned)(R * DM + C) * 2u;
    const int b = tid * 16 + i * 8192, kb = b >> 11, nb = (b >> 8) & 7, rs = (b >> 5) & 7, nh = (b >> 4) & 1, kr = rs ^ ((kb & 1) << 2);
    voffB[i] = (unsigned)((8 * kb + kr) * 4096 + 16 * nb + 8 * nh) * 2u;
  }
  const unsigned ldsw = (unsigned)wid * 1024u;
  const int aoff = lds_byte(wr * 64 + fr, fq * 8);
  const int boff0 = fq * 2048 + wc * 512 + ((((lane >> 2) & 3) ^ ((fq & 1) << 2)) * 32) + 8 * (lane & 3), boff1 = boff0 ^ 128;
  const char* cA = (const char*)A + (size_t)pm * 256 * DM * 2;
  const char* cB = (const char*)W + (size_t)pn * 256 * 2;
  constexpr size_t AK = 64 * 2, AH = (size_t)128 * DM * 2, BK_ = (size_t)64 * 4096 * 2, BH = 128 * 2;
#define SA(b, h) (((b) * 2 + (h)) * K1_HTB)
#define SB(b, h) ((4 + (b) * 2 + (h)) * K1_HTB)
#define STAGE(bufoff, gbase, voff) do { _Pragma("unroll") for (int _i = 0; _i < 2; ++_i) \
    __builtin_amdgcn_global_load_lds((const unsigned*)((const char*)(gbase) + (voff)[_i]), (LAS unsigned*)(lds + (bufoff) + ldsw + _i * 8192), 16, 0, 0); } while (0)
#define STA(b, h, kt) STAGE(SA(b, h), cA + (h) * AH + (kt) * AK, voffA)
#define STB(b, h, kt) STAGE(SB(b, h), cB + (h) * BH + (kt) * BK_, voffB)
#define LDA(dst, b, h) do { _Pragma("unroll") for (int m = 0; m < 4; ++m) _Pragma("unroll") for (int k = 0; k < 2; ++k) dst[m][k] = *(const LAS bf16x8*)(lds + SA(b, h) + aoff + m * 2048 + k * 1024); } while (0)
#define LDB(dst, b, h) do { _Pragma("unroll") for (int n = 0; n < 2; ++n) _Pragma("unroll") for (int k = 0; k < 2; ++k) \
    dst[n][k] = vtr2(lds + SB(b, h) + boff0 + k * 8192 + n * 256, lds + SB(b, h) + boff1 + k * 8192 + n * 256); } while (0)
#define MMA(ai, bj, At, Bt) do { __builtin_amdgcn_s_setprio(1); _Pragma("unroll") for (int m = 0; m < 4; ++m) _Pragma("unroll") for (int n = 0; n < 2; ++n) _Pragma("unroll") for (int k = 0; k < 2; ++k) \
    acc[ai][bj][m][n] = ISV ? MFMA16(At[m][k], Bt[n][k], acc[ai][bj][m][n]) : MFMA16(Bt[n][k], At[m][k], acc[ai][bj][m][n]); __builtin_amdgcn_s_setprio(0); } while (0)
  f32x4 acc[2][2][4][2];
#pragma unroll
  for (int a = 0; a < 2; ++a)
#pragma unroll
    for (int b = 0; b < 2; ++b)
#pragma unroll
      for (int m = 0; m < 4; ++m)
#pragma unroll
        for (int n = 0; n < 2; ++n) acc[a][b][m][n] = (f32x4){0.f, 0.f, 0.f, 0.f};
  bf16x8 At[4][2], B0[2][2], B1[2][2];
  constexpr int nt = DM / 64;
  STB(0, 0, 0); STA(0, 0, 0); STB(0, 1, 0); STA(0, 1, 0);
  if (wr == 1) BAR;
  asm volatile("s_waitcnt vmcnt(4)" : "+v"(xv) :: "memory");
  BAR;
  {
    float v[8];
#pragma unroll
    for (int i = 0; i < 4; ++i) { v[2 * i] = bflo(xv[i]); v[2 * i + 1] = bfhi(xv[i]); }
    float s = 0.f;
#pragma unroll
    for (int i = 0; i < 8; ++i) s += v[i];
#pragma unroll
    for (int o = 32; o >= 1; o >>= 1) s += __shfl_xor(s, o);
    const float mean = s * (1.0f / DM);
    float q = 0.f;
#pragma unroll
    for (int i = 0; i < 8; ++i) { const float d = v[i] - mean; q += d * d; }
#pragma unroll
    for (int o = 32; o >= 1; o >>= 1) q += __shfl_xor(q, o);
    const float mb = rbf(mean), vb = rbf(q * (1.0f / DM));
    const float den = rbf(sqrtf(rbf(vb + 1e-5f)));
    u32x4 o4;
#pragma unroll
    for (int i = 0; i < 4; ++i) o4[i] = cvtpk(rbf(v[2 * i] - mb) / den, rbf(v[2 * i + 1] - mb) / den);
    *(u32x4*)(xn_t + ((size_t)((row >> 5) * 32 + (lane >> 1)) * 64 + (lane & 1) * 32 + (row & 31)) * 8) = o4;
  }
  STB(1, 0, 1); STA(1, 0, 1); STB(1, 1, 1);
  WAIT_V(6); BAR;
  for (int t = 0; t < nt - 2; t += 2) {
    LDB(B0, 0, 0); SCHED; LDA(At, 0, 0); STA(1, 1, t + 1);
    WAIT_L(8); BAR; WAIT_L(0); MMA(0, 0, At, B0); BAR; SCHED;
    LDB(B1, 0, 1); STB(0, 0, t + 2);
    BAR; WAIT_L(0); MMA(0, 1, At, B1); BAR;
    LDA(At, 0, 1); STA(0, 0, t + 2);
    BAR; WAIT_L(0); MMA(1, 0, At, B0); BAR; SCHED;
    STB(0, 1, t + 2);
    WAIT_V(6); BAR; MMA(1, 1, At, B1); BAR;
    LDB(B0, 1, 0); SCHED; LDA(At, 1, 0); STA(0, 1, t + 2);
    WAIT_L(8); BAR; WAIT_L(0); MMA(0, 0, At, B0); BAR; SCHED;
    LDB(B1, 1, 1); STB(1, 0, t + 3);
    BAR; WAIT_L(0); MMA(0, 1, At, B1); BAR;
    LDA(At, 1, 1); STA(1, 0, t + 3);
    BAR; WAIT_L(0); MMA(1, 0, At, B0); BAR; SCHED;
    STB(1, 1, t + 3);
    WAIT_V(6); BAR; MMA(1, 1, At, B1); BAR;
  }
  { LDB(B0, 0, 0); WAIT_V(0); LDA(At, 0, 0); STA(1, 1, nt - 1);
    BAR; WAIT_L(0); MMA(0, 0, At, B0); BAR;
    LDB(B1, 0, 1); BAR; WAIT_L(0); MMA(0, 1, At, B1); BAR;
    LDA(At, 0, 1); WAIT_V(4); BAR; WAIT_L(0); MMA(1, 0, At, B0); MMA(1, 1, At, B1); BAR; }
  { LDB(B0, 1, 0); LDA(At, 1, 0); WAIT_V(2); BAR; WAIT_L(0); MMA(0, 0, At, B0); BAR;
    LDB(B1, 1, 1); WAIT_V(0); BAR; WAIT_L(0); MMA(0, 1, At, B1); BAR;
    LDA(At, 1, 1); BAR; WAIT_L(0); MMA(1, 0, At, B0); MMA(1, 1, At, B1); BAR; }
  if (wr == 0) BAR;
#pragma unroll
  for (int ai = 0; ai < 2; ++ai)
#pragma unroll
    for (int bj = 0; bj < 2; ++bj) {
      const int head = 2 * pn + bj, tile = 4 * pm + 2 * ai + wr;
      u16* tb = out + (size_t)(head * 32 + tile) * 8192;
      if (!ISV) {
#pragma unroll
        for (int m = 0; m < 4; ++m) {
          const f32x4 a0 = acc[ai][bj][m][0], a1 = acc[ai][bj][m][1];
          u32x4 w = {cvtpk(a0[0], a0[1]), cvtpk(a0[2], a0[3]), cvtpk(a1[0], a1[1]), cvtpk(a1[2], a1[3])};
          *(u32x4*)(tb + ((4 * wc + fq) * 64 + 16 * m + fr) * 8) = w;
        }
      } else {
#pragma unroll
        for (int mp = 0; mp < 2; ++mp)
#pragma unroll
          for (int n = 0; n < 2; ++n) {
            const f32x4 a0 = acc[ai][bj][2 * mp][n], a1 = acc[ai][bj][2 * mp + 1][n];
            u32x4 w = {cvtpk(a0[0], a0[1]), cvtpk(a0[2], a0[3]), cvtpk(a1[0], a1[1]), cvtpk(a1[2], a1[3])};
            *(u32x4*)(tb + ((4 * mp + fq) * 128 + 32 * wc + 16 * n + fr) * 8) = w;
          }
      }
    }
#undef SA
#undef SB
#undef STAGE
#undef STA
#undef STB
#undef LDA
#undef LDB
#undef MMA
}

__global__ __launch_bounds__(512, 2) void k1_kv(const u16* __restrict__ x, const u16* __restrict__ ckv, const u16* __restrict__ wuk, const u16* __restrict__ wuv,
                                                u16* __restrict__ Kws, u16* __restrict__ Vws, u16* __restrict__ xn_t, unsigned* __restrict__ cnt) {
  extern __shared__ __attribute__((aligned(16))) char smem[];
  const int b = blockIdx.x, xcd = b & 7, idx = b >> 3;
  const int pnn = xcd * 4 + (idx >> 3), pm = idx & 7;
  const int row = b * 8 + rfl(threadIdx.x >> 6);
  if (b == 0 && threadIdx.x < 64) cnt[threadIdx.x] = 0u;
  if (pnn < 16) k1_body<false>(ckv, wuk, Kws, pm, pnn, (LAS char*)smem, x + (size_t)row * DM, xn_t, row);
  else          k1_body<true>(ckv, wuv, Vws, pm, pnn - 16, (LAS char*)smem, x + (size_t)row * DM, xn_t, row);
}

constexpr int SLOT = 16384, K2_LDS = 10 * SLOT, CBUF = 3 * SLOT;
constexpr float QSCALE = 0.08838834764831845f * 1.4426950408889634f;
constexpr float THR = 8.f;
#define WBAR(N) do { asm volatile("s_waitcnt vmcnt(" #N ") lgkmcnt(0)\n\ts_barrier" ::: "memory"); SCHED; } while (0)
#define LBAR() do { asm volatile("s_waitcnt lgkmcnt(0)\n\ts_barrier" ::: "memory"); SCHED; } while (0)

__global__ __launch_bounds__(512, 2) void k2_attn(const u16* __restrict__ xn_t, const u16* __restrict__ Wq, const u16* __restrict__ W1, const u16* __restrict__ b1,
                                                  const u16* __restrict__ Kws, const u16* __restrict__ Vws, u16* __restrict__ Abuf) {
  extern __shared__ __attribute__((aligned(16))) char smem[];
  LAS char* lds = (LAS char*)smem;
  const unsigned lds0 = (unsigned)(uintptr_t)lds;
  const int tid = threadIdx.x, lane = tid & 63, wid = rfl(tid >> 6), grp = wid >> 2, wq = wid & 3, r = lane & 31, hh = lane >> 5;
  const int b = blockIdx.x, head = (b & 7) * 4 + (b >> 6), x = (b >> 3) & 7;
  const bool doF = head < 28;
  const int fh = doF ? head : 27;
  const int sw = 2 * x + 2;
  const int blk0 = grp ? 15 - x : x;
  const char* Kh = (const char*)Kws + (size_t)head * 32 * SLOT;
  const char* Vh = (const char*)Vws + (size_t)head * 32 * SLOT;
  const unsigned vlin = (unsigned)(wq * 4096 + lane * 16);
  const int tb0 = grp ? 15 - 2 * x : 0;

  bf16x8 qf[8];
  {
    const u16* xp = xn_t + (size_t)(blk0 * 4 + wq) * 32 * 512 + lane * 8;
    const int drow = (lane >> 2) & 7, dsub = lane >> 5, dc = lane & 3;
    const unsigned voffq = (unsigned)((16 * wid + drow) * 4096 + head * 128 + dsub * 32 + dc * 8) * 2u;
    const unsigned voff1 = (unsigned)((16 * wid + drow) * FF + fh * 64 + dsub * 32 + dc * 8) * 2u;
    auto stage_w = [&](int c, int cb) {
      const char* q0 = (const char*)Wq + (size_t)c * 128 * 4096 * 2;
      const char* f0 = (const char*)W1 + (size_t)c * 128 * FF * 2;
      const unsigned d0 = lds0 + cb * CBUF + wid * 6144;
      dma1k(q0, voffq, d0); dma1k(q0 + 128, voffq, d0 + 1024); dma1k(f0, voff1, d0 + 2048);
      dma1k(q0 + 8 * 4096 * 2, voffq, d0 + 3072); dma1k(q0 + 8 * 4096 * 2 + 128, voffq, d0 + 4096); dma1k(f0 + 8 * FF * 2, voff1, d0 + 5120);
    };
    const int b16 = (lane >> 4) & 1, q4 = (lane >> 2) & 3, p4 = lane & 3;
    const int abQ = hh * 3072 + q4 * 64 + 2 * (16 * (p4 >> 1) + 8 * b16 + 4 * (p4 & 1));
    const int abF = hh * 3072 + q4 * 64 + 2 * (16 * b16 + 8 * (p4 & 1) + 4 * (p4 >> 1));
    f32x16 aq[4], af[2];
#pragma unroll
    for (int i = 0; i < 4; ++i) aq[i] = f32x16{};
    af[0] = f32x16{}; af[1] = f32x16{};
    bf16x8 xa[8], xb[8];
    auto compute = [&](int cb, const bf16x8* xf) {
      LAS const char* wq_ = lds + cb * CBUF + abQ;
      LAS const char* wf_ = lds + cb * CBUF + abF;
#pragma unroll
      for (int s8 = 0; s8 < 8; ++s8) {
#pragma unroll
        for (int c = 0; c < 4; ++c) aq[c] = MFMA32(vtr2(wq_ + s8 * 6144 + c * 512, wq_ + s8 * 6144 + c * 512 + 256), xf[s8], aq[c]);
        if (doF) {
#pragma unroll
          for (int c = 0; c < 2; ++c) af[c] = MFMA32(vtr2(wf_ + s8 * 6144 + (4 + c) * 512, wf_ + s8 * 6144 + (4 + c) * 512 + 256), xf[s8], af[c]);
        }
      }
    };
#pragma unroll
    for (int i = 0; i < 8; ++i) xa[i] = *(const bf16x8*)(xp + i * 512);
    stage_w(0, 0);
#pragma unroll
    for (int i = 0; i < 8; ++i) xb[i] = *(const bf16x8*)(xp + (8 + i) * 512);
    stage_w(1, 1); stage_w(2, 2);
    WBAR(12);
    compute(0, xa);
#pragma unroll
    for (int i = 0; i < 8; ++i) xa[i] = *(const bf16x8*)(xp + (16 + i) * 512);
    WBAR(6);
    stage_w(3, 0);
    compute(1, xb);
#pragma unroll
    for (int i = 0; i < 8; ++i) xb[i] = *(const bf16x8*)(xp + (24 + i) * 512);
    WBAR(6);
    if (grp == 0) {
      dma4k(Kh, vlin, lds0 + 3 * SLOT + wq * 4096); dma4k(Vh, vlin, lds0 + 4 * SLOT + wq * 4096); dma4k(Kh + (sw > 1 ? SLOT : 0), vlin, lds0 + 5 * SLOT + wq * 4096);
    }
    compute(2, xa);
    if (grp == 0) WBAR(12); else WBAR(0);
    if (grp == 1) {
      dma4k(Kh + (size_t)tb0 * SLOT, vlin, lds0 + 6 * SLOT + wq * 4096); dma4k(Vh + (size_t)tb0 * SLOT, vlin, lds0 + 7 * SLOT + wq * 4096);
      dma4k(Kh + (size_t)(tb0 + 1) * SLOT, vlin, lds0 + 8 * SLOT + wq * 4096);
    }
    compute(0, xb);
    WBAR(8);
#pragma unroll
    for (int g = 0; g < 4; ++g)
#pragma unroll
      for (int s = 0; s < 2; ++s) {
        u32x4 w;
#pragma unroll
        for (int j = 0; j < 4; ++j) w[j] = cvtpk(aq[g][8 * s + 2 * j] * QSCALE, aq[g][8 * s + 2 * j + 1] * QSCALE);
        qf[2 * g + s] = __builtin_bit_cast(bf16x8, w);
      }
    if (grp == 1) {
      LAS char* st = lds + (wq < 2 ? 2 * SLOT : 9 * SLOT) + (wq & 1) * 8192 + lane * 16;
#pragma unroll
      for (int i = 0; i < 8; ++i) *(LAS bf16x8*)(st + i * 1024) = qf[i];
    }
    if (doF) {
      u16* hp = Abuf + ((size_t)(blk0 * NKT + 64 + head) * 8 * 128 + 32 * wq + r) * 8;
#pragma unroll
      for (int fb = 0; fb < 2; ++fb)
#pragma unroll
        for (int s = 0; s < 2; ++s) {
          const u32x4 bb = *(const u32x4*)(b1 + head * 64 + 32 * fb + 16 * s + 8 * hh);
          u32x4 w;
#pragma unroll
          for (int j = 0; j < 4; ++j) {
            float v0 = rbf(rbf(af[fb][8 * s + 2 * j]) + bflo(bb[j])), v1 = rbf(rbf(af[fb][8 * s + 2 * j + 1]) + bfhi(bb[j]));
            v0 = v0 > 0.f ? v0 : 0.f; v1 = v1 > 0.f ? v1 : 0.f;
            w[j] = cvtpk(v0 * v0, v1 * v1);
          }
          *(u32x4*)(hp + (size_t)(4 * fb + 2 * s + hh) * 128 * 8) = w;
        }
    }
  }
  const int rs34 = (r & 7) | ((r & 8) << 1) | ((r & 16) >> 1);
  const int rs23 = (r & 19) | ((r & 4) << 1) | ((r & 8) >> 1);
  const int klane = hh * 1024 + rs34 * 16, vlane = hh * 2048 + rs23 * 16;
  const int kslot0 = (3 + 3 * grp) * SLOT, kslot1 = (5 + 3 * grp) * SLOT, vslot0 = (4 + 3 * grp) * SLOT, vslot1 = grp * SLOT;
  const int qrel = 32 * wq + r;
  f32x16 o[4], s0, s1, negm = f32x16{};
#pragma unroll
  for (int i = 0; i < 4; ++i) o[i] = f32x16{};
  bf16x8 pf[4];
#pragma unroll
  for (int i = 0; i < 4; ++i) pf[i] = bf16x8{};
  float mref = 0.f, lsum = 0.f;
  auto store_ctx = [&](int blk, float inv) {
    u16* cp = Abuf + ((size_t)(blk * NKT + 2 * head) * 8 * 128 + 32 * wq + r) * 8;
#pragma unroll
    for (int db = 0; db < 4; ++db)
#pragma unroll
      for (int s = 0; s < 2; ++s) {
        u32x4 w;
#pragma unroll
        for (int j = 0; j < 4; ++j) w[j] = cvtpk(o[db][8 * s + 2 * j] * inv, o[db][8 * s + 2 * j + 1] * inv);
        *(u32x4*)(cp + (size_t)((db >> 1) * 8 + 4 * (db & 1) + 2 * s + hh) * 128 * 8) = w;
      }
  };
  if (grp == 1) LBAR();
  for (int s = 0; s <= 17; ++s) {
    {
      LAS const char* kp = lds + ((s & 1) ? kslot1 : kslot0) + klane;
      LAS const char* vp = lds + ((s & 1) ? vslot0 : vslot1) + vlane;
      if (s <= 16) {
#pragma unroll
        for (int d0 = 0; d0 < 8; ++d0) {
          const bf16x8 k0 = *(LAS const bf16x8*)(kp + d0 * 2048), k1 = *(LAS const bf16x8*)(kp + d0 * 2048 + 512);
          s0 = MFMA32(k0, qf[d0], d0 == 0 ? negm : s0);
          s1 = MFMA32(k1, qf[d0], d0 == 0 ? negm : s1);
        }
      }
      if (s >= 1) {
#pragma unroll
        for (int ks = 0; ks < 4; ++ks)
#pragma unroll
          for (int db = 0; db < 4; ++db) o[db] = MFMA32(*(LAS const bf16x8*)(vp + ks * 4096 + db * 512), pf[ks], o[db]);
      }
    }
    if (s == 17) break;
    LBAR();
    const bool swst = (grp == 0) && (s == sw);
    {
      const int sk = s + 2, sv = s + 1;
      if (sk <= 16) { const int tk = grp ? tb0 + sk : (sk < sw ? sk : sk - sw); dma4k(Kh + (size_t)tk * SLOT, vlin, lds0 + ((s & 1) ? kslot1 : kslot0) + wq * 4096); }
      if (sv <= 16) { const int tv = grp ? tb0 + sv : (sv < sw ? sv : sv - sw); dma4k(Vh + (size_t)tv * SLOT, vlin, lds0 + ((s & 1) ? vslot0 : vslot1) + wq * 4096); }
    }
    if (swst) {
      auto rr = __builtin_amdgcn_permlane32_swap(__float_as_uint(lsum), __float_as_uint(lsum), false, false);
      const float lt = __uint_as_float(rr[0]) + __uint_as_float(rr[1]);
      store_ctx(x, 1.0f / lt);
#pragma unroll
      for (int i = 0; i < 4; ++i) o[i] = f32x16{};
      lsum = 0.f;
    }
    const bool first = (s == 0) || swst;
    const int jb = grp ? s - 15 : s - (sw - 2);
    if (jb >= 0 && jb <= 1) {
      const int dq = qrel - 64 * jb - 4 * hh;
#pragma unroll
      for (int i = 0; i < 16; ++i) {
        const int kl = 16 * ((i >> 2) & 1) + 8 * (i >> 3) + (i & 3);
        if (kl > dq) s0[i] = -__builtin_inff();
        if (kl + 32 > dq) s1[i] = -__builtin_inff();
      }
    }
    float pmax = fmaxf(s0[0], s1[0]);
#pragma unroll
    for (int i = 1; i < 16; ++i) pmax = fmaxf(fmaxf(pmax, s0[i]), s1[i]);
    { auto rr = __builtin_amdgcn_permlane32_swap(__float_as_uint(pmax), __float_as_uint(pmax), false, false);
      pmax = fmaxf(__uint_as_float(rr[0]), __uint_as_float(rr[1])); }
    if (first || __any(pmax > THR)) {
      const float dl = first ? pmax : fmaxf(pmax, 0.f);
      mref += dl;
#pragma unroll
      for (int i = 0; i < 16; ++i) { s0[i] -= dl; s1[i] -= dl; }
      if (!first) {
        const float al = __builtin_amdgcn_exp2f(-dl);
        lsum *= al;
#pragma unroll
        for (int d = 0; d < 4; ++d)
#pragma unroll
          for (int i = 0; i < 16; ++i) o[d][i] *= al;
      }
#pragma unroll
      for (int i = 0; i < 16; ++i) negm[i] = -mref;
    }
    {
      float a0 = 0.f, a1 = 0.f;
#pragma unroll
      for (int i = 0; i < 16; ++i) { s0[i] = __builtin_amdgcn_exp2f(s0[i]); s1[i] = __builtin_amdgcn_exp2f(s1[i]); a0 += s0[i]; a1 += s1[i]; }
      lsum += a0 + a1;
#pragma unroll
      for (int s2 = 0; s2 < 2; ++s2) {
        u32x4 w0, w1;
#pragma unroll
        for (int j = 0; j < 4; ++j) { w0[j] = cvtpk(s0[8 * s2 + 2 * j], s0[8 * s2 + 2 * j + 1]); w1[j] = cvtpk(s1[8 * s2 + 2 * j], s1[8 * s2 + 2 * j + 1]); }
        pf[s2] = __builtin_bit_cast(bf16x8, w0); pf[2 + s2] = __builtin_bit_cast(bf16x8, w1);
      }
    }
    if (grp == 0 && s == sw - 1) {
      LAS const char* st = lds + (wq < 2 ? 2 * SLOT : 9 * SLOT) + (wq & 1) * 8192 + lane * 16;
#pragma unroll
      for (int i = 0; i < 8; ++i) qf[i] = *(LAS const bf16x8*)(st + i * 1024);
      mref = 0.f; negm = f32x16{};
    }
    if (s <= 14) { if (swst) WBAR(16); else WBAR(8); } else if (s == 15) WBAR(4); else WBAR(0);
  }
  LAS char* pb = lds + (wq == 3 ? 0 : (3 + wq) * SLOT) + lane * 16;
  LAS char* mlp = lds + 2 * SLOT + wq * 512 + lane * 8;
  if (grp == 0) {
    LBAR();
#pragma unroll
    for (int d = 0; d < 4; ++d)
#pragma unroll
      for (int i = 0; i < 4; ++i) *(LAS f32x4*)(pb + (d * 4 + i) * 1024) = (f32x4){o[d][4 * i], o[d][4 * i + 1], o[d][4 * i + 2], o[d][4 * i + 3]};
    *(LAS f32x2*)mlp = (f32x2){mref, lsum};
    LBAR();
  } else {
    LBAR();
    const f32x2 ml = *(LAS const f32x2*)mlp;
    const float mn = fmaxf(ml[0], mref), fa = __builtin_amdgcn_exp2f(ml[0] - mn), fb = __builtin_amdgcn_exp2f(mref - mn);
    float l2 = lsum * fb + ml[1] * fa;
    auto rr = __builtin_amdgcn_permlane32_swap(__float_as_uint(l2), __float_as_uint(l2), false, false);
    const float inv = 1.0f / (__uint_as_float(rr[0]) + __uint_as_float(rr[1]));
    const float ga = fa * inv, gb = fb * inv;
#pragma unroll
    for (int d = 0; d < 4; ++d)
#pragma unroll
      for (int i = 0; i < 4; ++i) {
        const f32x4 pa = *(LAS const f32x4*)(pb + (d * 4 + i) * 1024);
#pragma unroll
        for (int e = 0; e < 4; ++e) o[d][4 * i + e] = o[d][4 * i + e] * gb + pa[e] * ga;
      }
    store_ctx(15 - x, 1.0f);
  }
}

constexpr int K3_LDS = 4 * 32768 + 16;
__global__ __launch_bounds__(512, 2) void k3_out(const u16* __restrict__ Abuf, const u16* __restrict__ Wo, const u16* __restrict__ W2, const u16* __restrict__ x,
                                                 const u16* __restrict__ b2, float* __restrict__ slabs, unsigned* __restrict__ cnt, u16* __restrict__ out) {
  extern __shared__ __attribute__((aligned(16))) char smem[];
  LAS char* lds = (LAS char*)smem;
  const unsigned lds0 = (unsigned)(uintptr_t)lds;
  const int tid = threadIdx.x, lane = tid & 63, wid = rfl(tid >> 6), wr = wid >> 2, wc = wid & 3, fr = lane & 15, fq = lane >> 4;
  const int b = blockIdx.x, xcd = b & 7, idx = b >> 3, sp = xcd >> 1, rt = (xcd & 1) * 8 + (idx >> 2), ct = idx & 3;
  const int kt0 = sp * 23;
  const char* Ab = (const char*)Abuf + (size_t)(rt * NKT + kt0) * 16384;
  const unsigned voffa = (unsigned)(wid * 2048 + lane * 16);
  const unsigned voffb = (unsigned)((8 * wid + (((lane >> 1) & 7) ^ ((wid & 1) << 2))) * DM + 128 * ct + 16 * (lane >> 4) + 8 * (lane & 1)) * 2u;
  auto stage = [&](int it) {
    const int kt = kt0 + it;
    const char* wb = kt < 64 ? (const char*)Wo + (size_t)kt * 64 * DM * 2 : (const char*)W2 + (size_t)(kt - 64) * 64 * DM * 2;
    const unsigned d = lds0 + (it & 3) * 32768 + wid * 2048;
    dma2k(Ab + (size_t)it * 16384, voffa, d);
    dma1k(wb, voffb, d + 16384); dma1k(wb + 128, voffb, d + 16384 + 1024);
  };
  const int aoff = fq * 2048 + (wr * 64 + fr) * 16;
  const int boff0 = 16384 + fq * 2048 + wc * 512 + ((((lane >> 2) & 3) ^ ((fq & 1) << 2)) * 32) + 8 * (lane & 3), boff1 = boff0 ^ 128;
  f32x4 acc[4][2];
#pragma unroll
  for (int m = 0; m < 4; ++m) { acc[m][0] = (f32x4){0.f, 0.f, 0.f, 0.f}; acc[m][1] = (f32x4){0.f, 0.f, 0.f, 0.f}; }
  stage(0); stage(1); stage(2);
  for (int it = 0; it < 23; ++it) {
    if (it <= 20) WBAR(8); else if (it == 21) WBAR(4); else WBAR(0);
    if (it + 3 < 23) stage(it + 3);
    LAS const char* sb = lds + (it & 3) * 32768;
    bf16x8 af[4][2], bf[2][2];
#pragma unroll
    for (int n = 0; n < 2; ++n)
#pragma unroll
      for (int k = 0; k < 2; ++k) bf[n][k] = vtr2(sb + boff0 + k * 8192 + n * 256, sb + boff1 + k * 8192 + n * 256);
#pragma unroll
    for (int m = 0; m < 4; ++m)
#pragma unroll
      for (int k = 0; k < 2; ++k) af[m][k] = *(LAS const bf16x8*)(sb + aoff + k * 8192 + m * 256);
#pragma unroll
    for (int k = 0; k < 2; ++k)
#pragma unroll
      for (int m = 0; m < 4; ++m)
#pragma unroll
        for (int n = 0; n < 2; ++n) acc[m][n] = MFMA16(bf[n][k], af[m][k], acc[m][n]);
  }
  const int tile = rt * 4 + ct;
  const __amdgpu_buffer_rsrc_t rs = __builtin_amdgcn_make_buffer_rsrc((void*)(slabs + (size_t)tile * 4 * 16384), (short)0, 4 * 65536, 0x00020000);
  const int sl = (wid * 8) * 1024 + lane * 16;
#pragma unroll
  for (int m = 0; m < 4; ++m)
#pragma unroll
    for (int n = 0; n < 2; ++n) __builtin_amdgcn_raw_buffer_store_b128(__builtin_bit_cast(u32x4, acc[m][n]), rs, sp * 65536 + sl + (m * 2 + n) * 1024, 0, 16);
  asm volatile("s_waitcnt vmcnt(0)" ::: "memory");
  __syncthreads();
  LAS unsigned* flag = (LAS unsigned*)(lds + 4 * 32768);
  if (tid == 0) {
    const unsigned old = __hip_atomic_fetch_add(cnt + tile, 1u, __ATOMIC_RELAXED, __HIP_MEMORY_SCOPE_AGENT);
    if (old == 3u) { __builtin_amdgcn_fence(__ATOMIC_ACQUIRE, "agent"); asm volatile("s_waitcnt vmcnt(0)" ::: "memory"); }
    *flag = old;
  }
  __syncthreads();
  if (*flag != 3u) return;
#pragma unroll
  for (int o = 1; o < 4; ++o) {
    const int os = (sp + o) & 3;
#pragma unroll
    for (int m = 0; m < 4; ++m)
#pragma unroll
      for (int n = 0; n < 2; ++n)
        acc[m][n] += __builtin_bit_cast(f32x4, __builtin_amdgcn_raw_buffer_load_b128(rs, os * 65536 + sl + (m * 2 + n) * 1024, 0, 16));
  }
#pragma unroll
  for (int n = 0; n < 2; ++n) {
    const int col = ct * 128 + 32 * wc + 16 * n + 4 * fq;
    const u32x2 bb = *(const u32x2*)(b2 + col);
#pragma unroll
    for (int m = 0; m < 4; ++m) {
      const size_t off = (size_t)(rt * 128 + 64 * wr + 16 * m + fr) * DM + col;
      const u32x2 xx = *(const u32x2*)(x + off);
      const f32x4 a = acc[m][n];
      u32x2 w = {cvtpk(a[0] + bflo(xx[0]) + bflo(bb[0]), a[1] + bfhi(xx[0]) + bfhi(bb[0])), cvtpk(a[2] + bflo(xx[1]) + bflo(bb[1]), a[3] + bfhi(xx[1]) + bfhi(bb[1]))};
      *(u32x2*)(out + off) = w;
    }
  }
}

extern "C" void kernel_launch(void* const* d_in, const int* in_sizes, int n_in, void* d_out, int out_size, void* d_ws, size_t ws_size,
                              hipStream_t stream) {
  const u16* x = (const u16*)d_in[0];
  const u16* ckv = (const u16*)d_in[1];
  const u16* wq = (const u16*)d_in[2];
  const u16* wuk = (const u16*)d_in[3];
  const u16* wuv = (const u16*)d_in[4];
  const u16* wo = (const u16*)d_in[5];
  const u16* w1 = (const u16*)d_in[7];
  const u16* b1 = (const u16*)d_in[8];
  const u16* w2 = (const u16*)d_in[9];
  const u16* b2 = (const u16*)d_in[10];
  char* ws = (char*)d_ws;
  u16* Kws = (u16*)(ws + OFF_K); u16* Vws = (u16*)(ws + OFF_V); u16* xn_t = (u16*)(ws + OFF_XN); u16* Abuf = (u16*)(ws + OFF_A);
  float* slabs = (float*)(ws + OFF_SLAB); unsigned* cnt = (unsigned*)(ws + OFF_CNT);
  (void)hipFuncSetAttribute((const void*)k1_kv, hipFuncAttributeMaxDynamicSharedMemorySize, K1_LDS);
  (void)hipFuncSetAttribute((const void*)k2_attn, hipFuncAttributeMaxDynamicSharedMemorySize, K2_LDS);
  (void)hipFuncSetAttribute((const void*)k3_out, hipFuncAttributeMaxDynamicSharedMemorySize, K3_LDS);
  k1_kv<<<256, 512, K1_LDS, stream>>>(x, ckv, wuk, wuv, Kws, Vws, xn_t, cnt);
  k2_attn<<<256, 512, K2_LDS, stream>>>(xn_t, wq, w1, b1, Kws, Vws, Abuf);
  k3_out<<<256, 512, K3_LDS, stream>>>(Abuf, wo, w2, x, b2, slabs, cnt, (u16*)d_out);
}
```

```cpp
#include <hip/hip_runtime.h>
#include <hip/hip_bf16.h>
#include <stdint.h>
#include <type_traits>

typedef unsigned short u16;
typedef short bf16x8 __attribute__((ext_vector_type(8)));
typedef short s16x4 __attribute__((ext_vector_type(4)));
typedef short v4i16_t __attribute__((ext_vector_type(4)));
typedef float f32x2 __attribute__((ext_vector_type(2)));
typedef float f32x4 __attribute__((ext_vector_type(4)));
typedef float f32x16 __attribute__((ext_vector_type(16)));
typedef unsigned u32x4 __attribute__((ext_vector_type(4)));
typedef unsigned u32x2 __attribute__((ext_vector_type(2)));
typedef __bf16 bf16x2_t __attribute__((ext_vector_type(2)));
#define LAS __attribute__((address_space(3)))
#define DI __device__ __forceinline__

constexpr int SEQ = 2048, DM = 512, NH = 32, HD = 128, FF = 1792, KTOT = NH * HD + FF, NKT = KTOT / 64;
constexpr size_t MiB = 1024 * 1024;
#define WT_AUX 0
constexpr unsigned SENT = 0xffffffffu;
constexpr size_t OFF_K = 0, OFF_V = 16 * MiB, OFF_XN = 32 * MiB, OFF_A = 34 * MiB, OFF_SLAB = 58 * MiB, OFF_CNT = 74 * MiB;

DI unsigned cvtpk(float lo, float hi) { f32x2 v = {lo, hi}; bf16x2_t b = __builtin_convertvector(v, bf16x2_t); return __builtin_bit_cast(unsigned, b); }
DI float rbf(float x) { return __uint_as_float(cvtpk(x, x) & 0xffff0000u); }
DI float bflo(unsigned w) { return __uint_as_float(w << 16); }
DI float bfhi(unsigned w) { return __uint_as_float(w & 0xffff0000u); }
DI s16x4 vtr(LAS const char* p) { return __builtin_bit_cast(s16x4, __builtin_amdgcn_ds_read_tr16_b64_v4i16((LAS v4i16_t*)p)); }
DI bf16x8 vtr2(LAS const char* p0, LAS const char* p1) { s16x4 a = vtr(p0), b = vtr(p1); return __builtin_shufflevector(a, b, 0, 1, 2, 3, 4, 5, 6, 7); }
DI float max3f(float a, float b, float c) { float r; asm("v_max3_f32 %0, %1, %2, %3" : "=v"(r) : "v"(a), "v"(b), "v"(c)); return r; }
DI float max2f(float a, float b) { float r; asm("v_max_f32_e32 %0, %1, %2" : "=v"(r) : "v"(a), "v"(b)); return r; }
DI float fadd_s(float a, float b) { float r; asm("v_add_f32_e32 %0, %1, %2" : "=v"(r) : "v"(a), "v"(b)); return r; }
DI int rfl(int x) { return __builtin_amdgcn_readfirstlane(x); }
#define SCHED __builtin_amdgcn_sched_barrier(0)
#define WAIT_V(n) asm volatile("s_waitcnt vmcnt(" #n ")" ::: "memory")
#define WAIT_L(n) asm volatile("s_waitcnt lgkmcnt(" #n ")" ::: "memory")
#define BAR __builtin_amdgcn_s_barrier()
#define EXP2(x) __builtin_amdgcn_exp2f(x)
#define MFMA32(a, b, c) __builtin_amdgcn_mfma_f32_32x32x16_bf16((a), (b), (c), 0, 0, 0)
#define MFMA16(a, b, c) __builtin_amdgcn_mfma_f32_16x16x32_bf16((a), (b), (c), 0, 0, 0)
#define STAMP(kid, i) do {} while (0)
#define STAMPR(kid, i) do {} while (0)

DI void dma1k(const void* sbase, unsigned voff, unsigned ldsdst) {
  unsigned sv;
  asm volatile("s_mov_b32 %0, m0\n\ts_mov_b32 m0, %3\n\ts_nop 0\n\t"
               "global_load_lds_dwordx4 %1, %2\n\t"
               "s_mov_b32 m0, %0" : "=&s"(sv) : "v"(voff), "s"(sbase), "s"(ldsdst) : "memory");
}
DI void touch64(const void* p, unsigned ldsdump) {
  unsigned sv;
  asm volatile("s_mov_b32 %0, m0\n\ts_mov_b32 m0, %2\n\ts_nop 0\n\t"
               "global_load_lds_dword %1, off\n\t"
               "s_mov_b32 m0, %0" : "=&s"(sv) : "v"(p), "s"(ldsdump) : "memory");
}
DI void dma2k(const void* sbase, unsigned voff, unsigned ldsdst) {
  unsigned sv;
  asm volatile("s_mov_b32 %0, m0\n\ts_mov_b32 m0, %3\n\ts_nop 0\n\t"
               "global_load_lds_dwordx4 %1, %2\n\t"
               "global_load_lds_dwordx4 %1, %2 offset:1024\n\t"
               "s_mov_b32 m0, %0" : "=&s"(sv) : "v"(voff), "s"(sbase), "s"(ldsdst) : "memory");
}
DI void dma4k(const void* sbase, unsigned voff, unsigned ldsdst) {
  unsigned sv;
  asm volatile("s_mov_b32 %0, m0\n\ts_mov_b32 m0, %3\n\ts_nop 0\n\t"
               "global_load_lds_dwordx4 %1, %2\n\t"
               "global_load_lds_dwordx4 %1, %2 offset:1024\n\t"
               "global_load_lds_dwordx4 %1, %2 offset:2048\n\t"
               "global_load_lds_dwordx4 %1, %2 offset:3072\n\t"
               "s_mov_b32 m0, %0" : "=&s"(sv) : "v"(voff), "s"(sbase), "s"(ldsdst) : "memory");
}

constexpr int K1_HTB = 128 * 64 * 2, K1_LDS = 8 * K1_HTB;
DI int lds_byte(int r, int c) { const int st = (r >> 4) * 2 + (c >> 5), rr = r & 15, cc = c & 31, ob = rr * 64 + cc * 2; return st * 1024 + (ob ^ (((ob >> 9) & 1) << 5)); }
DI void stage_rc(int b, int& R, int& C) { const int st = b / 1024, sb = b % 1024, swz = sb ^ (((sb >> 9) & 1) << 5); R = (st >> 1) * 16 + swz / 64; C = (st & 1) * 32 + (swz % 64) / 2; }

template <bool ISV>
DI void k1_body(const u16* __restrict__ A, const u16* __restrict__ W, u16* __restrict__ out, int pm, int pn, LAS char* lds,
                const u16* __restrict__ xrow, u16* __restrict__ xn_t, int row, const char* __restrict__ warm_src) {
  const int tid = threadIdx.x, wid = rfl(tid >> 6), lane = tid & 63, wr = wid >> 2, wc = wid & 3, fr = lane & 15, fq = lane >> 4;
  STAMPR(0, 62); STAMP(0, 0);
  u32x4 xv;
  asm volatile("global_load_dwordx4 %0, %1, off" : "=v"(xv) : "v"(xrow + lane * 8) : "memory");
  unsigned voffA[2], voffB[2];
#pragma unroll
  for (int i = 0; i < 2; ++i) {
    int R, C; stage_rc(tid * 16 + i * 8192, R, C); voffA[i] = (unsigned)(R * DM + C) * 2u;
    const int b = tid * 16 + i * 8192, kb = b >> 11, nh = (b >> 10) & 1, kr = (b >> 7) & 7, c16 = (b >> 4) & 7;
    const int slot = (c16 >> 1) ^ (((kr >> 1) & 1) | ((kb & 1) << 1));
    voffB[i] = (unsigned)((8 * kb + kr) * 4096 + 64 * nh + 16 * slot + 8 * (c16 & 1)) * 2u;
  }
  const unsigned ldsw = (unsigned)wid * 1024u;
  const int aoff = lds_byte(wr * 64 + fr, fq * 8);
  const int boff0 = fq * 2048 + (wc >> 1) * 1024 + ((lane >> 2) & 3) * 128 + (((2 * (wc & 1)) ^ (((lane >> 3) & 1) | ((fq & 1) << 1))) * 32) + 8 * (lane & 3), boff1 = boff0 ^ 32;
  const char* cA = (const char*)A + (size_t)pm * 256 * DM * 2;
  const char* cB = (const char*)W + (size_t)pn * 256 * 2;
  constexpr size_t AK = 64 * 2, AH = (size_t)128 * DM * 2, BK_ = (size_t)64 * 4096 * 2, BH = 128 * 2;
#define SA(b, h) (((b) * 2 + (h)) * K1_HTB)
#define SB(b, h) ((4 + (b) * 2 + (h)) * K1_HTB)
#define STAGE(bufoff, gbase, voff) do { _Pragma("unroll") for (int _i = 0; _i < 2; ++_i) \
    __builtin_amdgcn_global_load_lds((const unsigned*)((const char*)(gbase) + (voff)[_i]), (LAS unsigned*)(lds + (bufoff) + ldsw + _i * 8192), 16, 0, 0); } while (0)
#define STA(b, h, kt) STAGE(SA(b, h), cA + (h) * AH + (kt) * AK, voffA)
#define STB(b, h, kt) STAGE(SB(b, h), cB + (h) * BH + (kt) * BK_, voffB)
#define LDA(dst, b, h) do { _Pragma("unroll") for (int m = 0; m < 4; ++m) _Pragma("unroll") for (int k = 0; k < 2; ++k) dst[m][k] = *(const LAS bf16x8*)(lds + SA(b, h) + aoff + m * 2048 + k * 1024); } while (0)
#define LDB(dst, b, h) do { _Pragma("unroll") for (int n = 0; n < 2; ++n) _Pragma("unroll") for (int k = 0; k < 2; ++k) \
    dst[n][k] = vtr2(lds + SB(b, h) + (n ? boff1 : boff0) + k * 8192, lds + SB(b, h) + (n ? boff1 : boff0) + k * 8192 + 512); } while (0)
#define MMA(ai, bj, At, Bt) do { __builtin_amdgcn_s_setprio(1); _Pragma("unroll") for (int m = 0; m < 4; ++m) _Pragma("unroll") for (int n = 0; n < 2; ++n) _Pragma("unroll") for (int k = 0; k < 2; ++k) \
    acc[ai][bj][m][n] = ISV ? MFMA16(At[m][k], Bt[n][k], acc[ai][bj][m][n]) : MFMA16(Bt[n][k], At[m][k], acc[ai][bj][m][n]); __builtin_amdgcn_s_setprio(0); } while (0)
  f32x4 acc[2][2][4][2];
#pragma unroll
  for (int a = 0; a < 2; ++a)
#pragma unroll
    for (int b = 0; b < 2; ++b)
#pragma unroll
      for (int m = 0; m < 4; ++m)
#pragma unroll
        for (int n = 0; n < 2; ++n) acc[a][b][m][n] = (f32x4){0.f, 0.f, 0.f, 0.f};
  bf16x8 At[4][2], B0[2][2], B1[2][2];
  constexpr int nt = DM / 64;
  STB(0, 0, 0); STA(0, 0, 0); STB(0, 1, 0); STA(0, 1, 0);
  if (wr == 1) BAR;
  asm volatile("s_waitcnt vmcnt(4)" : "+v"(xv) :: "memory");
  BAR;
  {
    float v[8];
#pragma unroll
    for (int i = 0; i < 4; ++i) { v[2 * i] = bflo(xv[i]); v[2 * i + 1] = bfhi(xv[i]); }
    float s = 0.f;
#pragma unroll
    for (int i = 0; i < 8; ++i) s += v[i];
#pragma unroll
    for (int o = 32; o >= 1; o >>= 1) s += __shfl_xor(s, o);
    const float mean = s * (1.0f / DM);
    float q = 0.f;
#pragma unroll
    for (int i = 0; i < 8; ++i) { const float d = v[i] - mean; q += d * d; }
#pragma unroll
    for (int o = 32; o >= 1; o >>= 1) q += __shfl_xor(q, o);
    const float mb = rbf(mean), vb = rbf(q * (1.0f / DM));
    const float den = rbf(sqrtf(rbf(vb + 1e-5f)));
    u32x4 o4;
#pragma unroll
    for (int i = 0; i < 4; ++i) o4[i] = cvtpk(rbf(v[2 * i] - mb) / den, rbf(v[2 * i + 1] - mb) / den);
    *(u32x4*)(xn_t + ((size_t)((row >> 5) * 32 + (lane >> 1)) * 64 + (lane & 1) * 32 + (row & 31)) * 8) = o4;
  }
  STB(1, 0, 1); STA(1, 0, 1); STB(1, 1, 1);
  WAIT_V(6); BAR;
  STAMP(0, 1);
  for (int t = 0; t < nt - 2; t += 2) {
    LDB(B0, 0, 0); SCHED; LDA(At, 0, 0); STA(1, 1, t + 1);
    WAIT_L(8); BAR; WAIT_L(0); MMA(0, 0, At, B0); BAR; SCHED;
    LDB(B1, 0, 1); STB(0, 0, t + 2);
    BAR; WAIT_L(0); MMA(0, 1, At, B1); BAR;
    LDA(At, 0, 1); STA(0, 0, t + 2);
    BAR; WAIT_L(0); MMA(1, 0, At, B0); BAR; SCHED;
    STB(0, 1, t + 2);
    WAIT_V(6); BAR; MMA(1, 1, At, B1); BAR;
    LDB(B0, 1, 0); SCHED; LDA(At, 1, 0); STA(0, 1, t + 2);
    WAIT_L(8); BAR; WAIT_L(0); MMA(0, 0, At, B0); BAR; SCHED;
    LDB(B1, 1, 1); STB(1, 0, t + 3);
    BAR; WAIT_L(0); MMA(0, 1, At, B1); BAR;
    LDA(At, 1, 1); STA(1, 0, t + 3);
    BAR; WAIT_L(0); MMA(1, 0, At, B0); BAR; SCHED;
    STB(1, 1, t + 3);
    WAIT_V(6); BAR; MMA(1, 1, At, B1); BAR;
  }
  { LDB(B0, 0, 0); WAIT_V(0); LDA(At, 0, 0); STA(1, 1, nt - 1);
    BAR; WAIT_L(0); MMA(0, 0, At, B0); BAR;
    LDB(B1, 0, 1); BAR; WAIT_L(0); MMA(0, 1, At, B1); BAR;
    LDA(At, 0, 1); WAIT_V(4); BAR; WAIT_L(0); MMA(1, 0, At, B0); MMA(1, 1, At, B1); BAR; }
  { LDB(B0, 1, 0); LDA(At, 1, 0); WAIT_V(2); BAR; WAIT_L(0); MMA(0, 0, At, B0); BAR;
    LDB(B1, 1, 1); WAIT_V(0); BAR; WAIT_L(0); MMA(0, 1, At, B1); BAR;
    LDA(At, 1, 1); BAR; WAIT_L(0); MMA(1, 0, At, B0); MMA(1, 1, At, B1); BAR; }
  if (wr == 0) BAR;
  STAMP(0, 2);
#pragma unroll
  for (int ai = 0; ai < 2; ++ai)
#pragma unroll
    for (int bj = 0; bj < 2; ++bj) {
      const int head = 2 * pn + bj, tile = 4 * pm + 2 * ai + wr;
      const __amdgpu_buffer_rsrc_t trs = __builtin_amdgcn_make_buffer_rsrc((void*)(out + (size_t)(head * 32 + tile) * 8192), (short)0, 16384, 0x00020000);
      if (!ISV) {
#pragma unroll
        for (int m = 0; m < 4; ++m) {
          const f32x4 a0 = acc[ai][bj][m][0], a1 = acc[ai][bj][m][1];
          u32x4 w = {cvtpk(a0[0], a0[1]), cvtpk(a0[2], a0[3]), cvtpk(a1[0], a1[1]), cvtpk(a1[2], a1[3])};
          __builtin_amdgcn_raw_buffer_store_b128(w, trs, ((4 * wc + fq) * 64 + 16 * m + fr) * 16, 0, WT_AUX);
        }
      } else {
#pragma unroll
        for (int mp = 0; mp < 2; ++mp)
#pragma unroll
          for (int n = 0; n < 2; ++n) {
            const f32x4 a0 = acc[ai][bj][2 * mp][n], a1 = acc[ai][bj][2 * mp + 1][n];
            u32x4 w = {cvtpk(a0[0], a0[1]), cvtpk(a0[2], a0[3]), cvtpk(a1[0], a1[1]), cvtpk(a1[2], a1[3])};
            __builtin_amdgcn_raw_buffer_store_b128(w, trs, ((4 * mp + fq) * 128 + 32 * wc + 16 * n + fr) * 16, 0, WT_AUX);
          }
      }
    }
  STAMP(0, 3); STAMPR(0, 63);
#undef SA
#undef SB
#undef STAGE
#undef STA
#undef STB
#undef LDA
#undef LDB
#undef MMA
}

__global__ __launch_bounds__(512, 2) void k1_kv(const u16* __restrict__ x, const u16* __restrict__ ckv, const u16* __restrict__ wuk, const u16* __restrict__ wuv,
                                                u16* __restrict__ Kws, u16* __restrict__ Vws, u16* __restrict__ xn_t, const u16* __restrict__ wq, const u16* __restrict__ w1) {
  extern __shared__ __attribute__((aligned(16))) char smem[];
  const int b = blockIdx.x, xcd = b & 7, idx = b >> 3;
  const int pnn = ((idx >> 4) << 4) + xcd * 2 + ((idx >> 3) & 1), pm = idx & 7;
  const int row = b * 8 + rfl(threadIdx.x >> 6);
  const int wv = rfl(threadIdx.x >> 6), ln = threadIdx.x & 63;
  const char* warm = nullptr;
  if (wv < 4) warm = (const char*)wq + (size_t)(16 * idx + 4 * wv + (ln >> 4)) * 8192 + xcd * 1024 + (ln & 15) * 64;
  else if (wv < 6 && xcd < 7) warm = (const char*)w1 + (size_t)(16 * idx + 8 * (wv - 4) + (ln >> 3)) * (FF * 2) + xcd * 512 + (ln & 7) * 64;
  if (pnn < 16) k1_body<false>(ckv, wuk, Kws, pm, pnn, (LAS char*)smem, x + (size_t)row * DM, xn_t, row, warm);
  else          k1_body<true>(ckv, wuv, Vws, pm, pnn - 16, (LAS char*)smem, x + (size_t)row * DM, xn_t, row, warm);
}

constexpr int SLOT = 16384, K2_LDS = 10 * SLOT, CBUF = 3 * SLOT;
constexpr float QSCALE = 0.08838834764831845f * 1.4426950408889634f;
constexpr float THR = 32.f;
#define WBAR(N) do { asm volatile("s_waitcnt vmcnt(" #N ") lgkmcnt(0)\n\ts_barrier" ::: "memory"); SCHED; } while (0)
#define LBAR() do { asm volatile("s_waitcnt lgkmcnt(0)\n\ts_barrier" ::: "memory"); SCHED; } while (0)

__global__ __launch_bounds__(512, 2) void k2_attn(const u16* __restrict__ xn_t, const u16* __restrict__ Wq, const u16* __restrict__ W1, const u16* __restrict__ b1,
                                                  const u16* __restrict__ Kws, const u16* __restrict__ Vws, u16* __restrict__ Abuf, float* __restrict__ slabs,
                                                  const u16* __restrict__ Wo, const u16* __restrict__ W2) {
  extern __shared__ __attribute__((aligned(16))) char smem[];
  LAS char* lds = (LAS char*)smem;
  const unsigned lds0 = (unsigned)(uintptr_t)lds;
  const int tid = threadIdx.x, lane = tid & 63, wid = rfl(tid >> 6), grp = wid >> 2, wq = wid & 3, r = lane & 31, hh = lane >> 5;
  const int b = blockIdx.x, head = (b & 7) * 4 + (b >> 6), x = (b >> 3) & 7;
  const bool doF = head < 28;
  const int fh = doF ? head : 27;
  const int sw = 2 * x + 2;
  const int blk0 = grp ? 15 - x : x;
  const char* Kh = (const char*)Kws + (size_t)head * 32 * SLOT;
  const char* Vh = (const char*)Vws + (size_t)head * 32 * SLOT;
  const unsigned vlin = (unsigned)(wq * 4096 + lane * 16);
  const int tb0 = grp ? 15 - 2 * x : 0;

  bf16x8 qf[8];
  STAMPR(1, 62); STAMP(1, 0);
  {
    const char* xbase = (const char*)xn_t + (size_t)(blk0 * 4 + wq) * 32768;
    const unsigned xvo = lane * 16;
    const int drow = (lane >> 2) & 7, dsub = lane >> 5, dc = lane & 3;
    const unsigned voffq = (unsigned)((8 * wid + drow) * 4096 + head * 128 + dsub * 32 + dc * 8) * 2u;
    const unsigned voff1 = (unsigned)((8 * wid + drow) * FF + fh * 64 + dsub * 32 + dc * 8) * 2u;
    constexpr int CH = 24576;
    auto stage_w = [&](int c) __attribute__((always_inline)) {
      const char* q0 = (const char*)Wq + (size_t)c * 64 * 4096 * 2;
      const char* f0 = (const char*)W1 + (size_t)c * 64 * FF * 2;
      const unsigned d0 = lds0 + (c & 3) * CH + wid * 3072;
      dma1k(q0, voffq, d0); dma1k(q0 + 128, voffq, d0 + 1024); dma1k(f0, voff1, d0 + 2048);
    };
    const int b16 = (lane >> 4) & 1, q4 = (lane >> 2) & 3, p4 = lane & 3;
    const int abQ = hh * 3072 + q4 * 64 + 2 * (16 * (p4 >> 1) + 8 * b16 + 4 * (p4 & 1));
    const int abF = hh * 3072 + q4 * 64 + 2 * (16 * b16 + 8 * (p4 & 1) + 4 * (p4 >> 1));
    f32x16 aq[4], af[2];
#pragma unroll
    for (int i = 0; i < 4; ++i) aq[i] = f32x16{};
    af[0] = f32x16{}; af[1] = f32x16{};
    bf16x8 xr[4][4];
#define XCH(c) (c)
#define XLOAD(c) asm volatile("global_load_dwordx4 %0, %4, %5\n\tglobal_load_dwordx4 %1, %4, %5 offset:1024\n\tglobal_load_dwordx4 %2, %4, %5 offset:2048\n\tglobal_load_dwordx4 %3, %4, %5 offset:3072" \
      : "=&v"(xr[(c) & 3][0]), "=&v"(xr[(c) & 3][1]), "=&v"(xr[(c) & 3][2]), "=&v"(xr[(c) & 3][3]) : "v"(xvo), "s"(xbase + XCH(c) * 4096) : "memory")
#define CWAIT(c, N) do { asm volatile("s_waitcnt vmcnt(" #N ") lgkmcnt(0)\n\ts_barrier" : "+v"(xr[(c) & 3][0]), "+v"(xr[(c) & 3][1]), "+v"(xr[(c) & 3][2]), "+v"(xr[(c) & 3][3]) :: "memory"); SCHED; } while (0)
    auto compute = [&](int c) __attribute__((always_inline)) {
      LAS const char* wq_ = lds + (c & 3) * CH + abQ;
      LAS const char* wf_ = lds + (c & 3) * CH + abF;
      bf16x8 wa[4][6];
#pragma unroll
      for (int j = 0; j < 4; ++j) wa[0][j] = vtr2(wq_ + j * 512, wq_ + j * 512 + 256);
      if (doF) { wa[0][4] = vtr2(wf_ + 2048, wf_ + 2048 + 256); wa[0][5] = vtr2(wf_ + 2560, wf_ + 2560 + 256); }
#pragma unroll
      for (int s4 = 0; s4 < 4; ++s4) {
#pragma unroll
        for (int j = 0; j < 4; ++j) {
          if (s4 < 3) wa[s4 + 1][j] = vtr2(wq_ + (s4 + 1) * 6144 + j * 512, wq_ + (s4 + 1) * 6144 + j * 512 + 256);
          aq[j] = MFMA32(wa[s4][j], xr[c & 3][s4], aq[j]);
          SCHED;
        }
        if (doF) {
#pragma unroll
          for (int j = 0; j < 2; ++j) {
            if (s4 < 3) wa[s4 + 1][4 + j] = vtr2(wf_ + (s4 + 1) * 6144 + (4 + j) * 512, wf_ + (s4 + 1) * 6144 + (4 + j) * 512 + 256);
            af[j] = MFMA32(wa[s4][4 + j], xr[c & 3][s4], af[j]);
            SCHED;
          }
        }
      }
    };
    XLOAD(0); stage_w(0); XLOAD(1); stage_w(1); XLOAD(2); stage_w(2);
    CWAIT(0, 14); STAMP(1, 1); XLOAD(3); stage_w(3); compute(0);
    CWAIT(1, 14); XLOAD(4); stage_w(4); compute(1);
    CWAIT(2, 14); XLOAD(5); stage_w(5); compute(2);
    CWAIT(3, 14); XLOAD(6); stage_w(6); compute(3);
    CWAIT(4, 14); XLOAD(7); stage_w(7); compute(4);
    CWAIT(5, 14);
    if (grp == 1) {
      dma4k(Kh + (size_t)tb0 * SLOT, vlin, lds0 + 6 * SLOT + wq * 4096); dma4k(Vh + (size_t)tb0 * SLOT, vlin, lds0 + 7 * SLOT + wq * 4096);
      dma4k(Kh + (size_t)(tb0 + 1) * SLOT, vlin, lds0 + 8 * SLOT + wq * 4096);
    }
    compute(5);
    if (grp == 0) CWAIT(6, 7); else CWAIT(6, 19);
    if (grp == 0) {
      dma4k(Kh, vlin, lds0 + 9 * SLOT + wq * 4096); dma4k(Vh, vlin, lds0 + 0 * SLOT + wq * 4096); dma4k(Kh + SLOT, vlin, lds0 + 1 * SLOT + wq * 4096);
    }
    compute(6);
    CWAIT(7, 12);
    compute(7);
    STAMP(1, 2);
    WBAR(8);
#undef XLOAD
#undef CWAIT
#pragma unroll
    for (int g = 0; g < 4; ++g)
#pragma unroll
      for (int s = 0; s < 2; ++s) {
        u32x4 w;
#pragma unroll
        for (int j = 0; j < 4; ++j) w[j] = cvtpk(aq[g][8 * s + 2 * j] * QSCALE, aq[g][8 * s + 2 * j + 1] * QSCALE);
        qf[2 * g + s] = __builtin_bit_cast(bf16x8, w);
      }
    if (grp == 1) {
      LAS char* st = lds + 4 * SLOT + wq * 8192 + lane * 16;
#pragma unroll
      for (int i = 0; i < 8; ++i) *(LAS bf16x8*)(st + i * 1024) = qf[i];
    }
    if (doF) {
      const __amdgpu_buffer_rsrc_t hrs = __builtin_amdgcn_make_buffer_rsrc((void*)(Abuf + (size_t)(blk0 * NKT + 64 + head) * 8192), (short)0, 16384, 0x00020000);
#pragma unroll
      for (int fb = 0; fb < 2; ++fb)
#pragma unroll
        for (int s = 0; s < 2; ++s) {
          const u32x4 bb = *(const u32x4*)(b1 + head * 64 + 32 * fb + 16 * s + 8 * hh);
          u32x4 w;
#pragma unroll
          for (int j = 0; j < 4; ++j) {
            float v0 = rbf(rbf(af[fb][8 * s + 2 * j]) + bflo(bb[j])), v1 = rbf(rbf(af[fb][8 * s + 2 * j + 1]) + bfhi(bb[j]));
            v0 = v0 > 0.f ? v0 : 0.f; v1 = v1 > 0.f ? v1 : 0.f;
            w[j] = cvtpk(v0 * v0, v1 * v1);
          }
          __builtin_amdgcn_raw_buffer_store_b128(w, hrs, ((4 * fb + 2 * s + hh) * 128 + 32 * wq + r) * 16, 0, WT_AUX);
        }
    }
  }
  {
    const __amdgpu_buffer_rsrc_t frs = __builtin_amdgcn_make_buffer_rsrc((void*)(slabs + (size_t)b * 12288), (short)0, 49152, 0x00020000);
#pragma unroll
    for (int i = 0; i < 6; ++i) __builtin_amdgcn_raw_buffer_store_b128((u32x4){SENT, SENT, SENT, SENT}, frs, tid * 16 + i * 8192, 0, 17);
  }
  const int rs34 = (r & 7) | ((r & 8) << 1) | ((r & 16) >> 1);
  const int rs23 = (r & 19) | ((r & 4) << 1) | ((r & 8) >> 1);
  const int klane = hh * 1024 + rs34 * 16, vlane = hh * 2048 + rs23 * 16;
  const int kslot0 = (grp ? 6 : 9) * SLOT, kslot1 = (grp ? 8 : 1) * SLOT, vslot0 = (grp ? 7 : 0) * SLOT, vslot1 = (grp ? 3 : 2) * SLOT;
  const int qrel = 32 * wq + r;
  f32x16 o[4], s0, s1, negm = f32x16{};
#pragma unroll
  for (int i = 0; i < 4; ++i) o[i] = f32x16{};
  bf16x8 pf[4];
#pragma unroll
  for (int i = 0; i < 4; ++i) pf[i] = bf16x8{};
  float mref = 0.f, lsum = 0.f;
  const __amdgpu_buffer_rsrc_t arsrc = __builtin_amdgcn_make_buffer_rsrc((void*)Abuf, (short)0, 16 * NKT * 16384, 0x00020000);
  const int cvoff = (32 * wq + r) * 16 + hh * 2048;
  auto store_ctx = [&](int blk, float inv) __attribute__((always_inline)) {
    const int sbase = (blk * NKT + 2 * head) * 16384;
#pragma unroll
    for (int db = 0; db < 4; ++db)
#pragma unroll
      for (int s = 0; s < 2; ++s) {
        u32x4 w;
#pragma unroll
        for (int j = 0; j < 4; ++j) w[j] = cvtpk(o[db][8 * s + 2 * j] * inv, o[db][8 * s + 2 * j + 1] * inv);
        __builtin_amdgcn_raw_buffer_store_b128(w, arsrc, cvoff + sbase + (4 * db + 2 * s) * 2048, 0, WT_AUX);
      }
  };
  constexpr int PD = 6;
  bf16x8 fr_[32];
  const int swk = grp ? 99 : sw;
  int ktile = tb0 + 1, vtile = tb0;
  unsigned kvo = 0, vvo = 0;
#define FRD(i) (((i) < 16) ? *(LAS const bf16x8*)(kp + ((i) >> 1) * 2048 + ((i) & 1) * 512) : *(LAS const bf16x8*)(vp + (((i) - 16) >> 2) * 4096 + (((i) - 16) & 3) * 512))
#define SETM0(v) asm volatile("s_mov_b32 m0, %0\n\ts_nop 0" :: "s"(v) : "memory")
#define DMAI(vo, base, off) asm volatile("global_load_lds_dwordx4 %0, %1 offset:" #off :: "v"(vo), "s"(base) : "memory")
#define MBODY(i) do { if (i < 16) { if (i & 1) s1 = MFMA32(fr_[i], qf[i >> 1], i < 2 ? negm : s1); else s0 = MFMA32(fr_[i], qf[i >> 1], i < 2 ? negm : s0); } \
      else o[(i - 16) & 3] = MFMA32(fr_[i], pf[(i - 16) >> 2], o[(i - 16) & 3]); } while (0)
#define KPIECE(s_, j) do { if ((j) == 0) { ktile = ((s_) + 2 == swk) ? 0 : ktile + 1; kvo = vlin + (unsigned)min(ktile, 31) * SLOT; SETM0(lds0 + (((s_) & 1) ? kslot1 : kslot0) + wq * 4096); DMAI(kvo, Kh, 0); } \
    if ((j) == 1) DMAI(kvo, Kh, 1024); if ((j) == 2) DMAI(kvo, Kh, 2048); if ((j) == 3) DMAI(kvo, Kh, 3072); } while (0)
#define VPIECE(s_, j) do { if ((j) == 0) { SETM0(lds0 + (((s_) & 1) ? vslot1 : vslot0) + wq * 4096); DMAI(vvo, Vh, 0); } \
    if ((j) == 1) DMAI(vvo, Vh, 1024); if ((j) == 2) DMAI(vvo, Vh, 2048); if ((j) == 3) DMAI(vvo, Vh, 3072); } while (0)
#define MPRE(s_, I0) do { \
    LAS const char* kp = lds + (((s_) & 1) ? kslot1 : kslot0) + klane; \
    LAS const char* vp = lds + (((s_) & 1) ? vslot0 : vslot1) + vlane; \
    _Pragma("unroll") for (int i = I0; i < I0 + PD; ++i) fr_[i] = FRD(i); \
    (void)kp; (void)vp; } while (0)
#define MSLOT(s_, I0, I1, DM) do { \
    LAS const char* kp = lds + (((s_) & 1) ? kslot1 : kslot0) + klane; \
    LAS const char* vp = lds + (((s_) & 1) ? vslot0 : vslot1) + vlane; \
    if (DM) { vtile = ((s_) == swk) ? 0 : vtile + 1; vvo = vlin + (unsigned)min(vtile, 31) * SLOT; } \
    _Pragma("unroll") for (int i = I0; i < I1; ++i) { \
      if (i + PD < I1) fr_[i + PD] = FRD(i + PD); \
      MBODY(i); \
      if ((DM) && (i & 3) == 2 && i < 16) VPIECE(s_, i >> 2); \
      SCHED; \
    } (void)kp; (void)vp; } while (0)
  auto sslot = [&](const int s, const bool SLOW) __attribute__((always_inline)) {
    KPIECE(s, 0);
    bool first = false;
    if (__builtin_expect(SLOW, 0)) {
      const bool swst = (s == swk);
      if (swst) {
        auto rr = __builtin_amdgcn_permlane32_swap(__float_as_uint(lsum), __float_as_uint(lsum), false, false);
        const float lt = __uint_as_float(rr[0]) + __uint_as_float(rr[1]);
        store_ctx(x, 1.0f / lt);
#pragma unroll
        for (int i = 0; i < 4; ++i) o[i] = f32x16{};
        lsum = 0.f;
      }
      first = (s == 0) || swst;
      const int jb = grp ? s - 15 : s - (sw - 2);
      if (jb >= 0 && jb <= 1) {
        const int dq = qrel - 64 * jb - 4 * hh;
#pragma unroll
        for (int i = 0; i < 16; ++i) {
          const int kl = 16 * ((i >> 2) & 1) + 8 * (i >> 3) + (i & 3);
          if (kl > dq) s0[i] = -__builtin_inff();
          if (kl + 32 > dq) s1[i] = -__builtin_inff();
        }
      }
    }
    float pm0 = max3f(s0[0], s0[1], s1[0]), pm1 = max3f(s0[2], s0[3], s1[1]);
    pm0 = max3f(pm0, s1[2], s1[3]);
#pragma unroll
    for (int i = 4; i < 16; i += 4) {
      pm0 = max3f(pm0, s0[i], s0[i + 1]); pm1 = max3f(pm1, s0[i + 2], s0[i + 3]);
      pm0 = max3f(pm0, s1[i], s1[i + 1]); pm1 = max3f(pm1, s1[i + 2], s1[i + 3]);
    }
    float pmax = max2f(pm0, pm1);
    KPIECE(s, 1);
    { auto rr = __builtin_amdgcn_permlane32_swap(__float_as_uint(pmax), __float_as_uint(pmax), false, false);
      pmax = max2f(__uint_as_float(rr[0]), __uint_as_float(rr[1])); }
    if (__builtin_expect(first || __any(pmax > THR), 0)) {
      const float dl = first ? pmax : fmaxf(pmax, 0.f);
      mref += dl;
#pragma unroll
      for (int i = 0; i < 16; ++i) { s0[i] -= dl; s1[i] -= dl; }
      if (!first) {
        const float al = __builtin_amdgcn_exp2f(-dl);
        lsum *= al;
#pragma unroll
        for (int d = 0; d < 4; ++d)
#pragma unroll
          for (int i = 0; i < 16; ++i) o[d][i] *= al;
      }
#pragma unroll
      for (int i = 0; i < 16; ++i) negm[i] = -mref;
    }
    {
      float a0 = lsum, a1 = 0.f;
      unsigned pw[16];
      s0[0] = EXP2(s0[0]); SCHED;
#pragma unroll
      for (int i = 0; i < 32; ++i) {
        if (i + 1 < 32) { if (i + 1 < 16) s0[i + 1] = EXP2(s0[i + 1]); else s1[i + 1 - 16] = EXP2(s1[i + 1 - 16]); }
        const float e = i < 16 ? s0[i] : s1[i - 16];
        if (i & 1) { a1 = fadd_s(a1, e); pw[i >> 1] = cvtpk(i < 16 ? s0[i - 1] : s1[i - 17], e); } else a0 = fadd_s(a0, e);
        if (i == 8) KPIECE(s, 2);
        if (i == 20) KPIECE(s, 3);
        SCHED;
      }
      lsum = fadd_s(a0, a1);
#pragma unroll
      for (int k4 = 0; k4 < 4; ++k4) pf[k4] = __builtin_bit_cast(bf16x8, (u32x4){pw[4 * k4], pw[4 * k4 + 1], pw[4 * k4 + 2], pw[4 * k4 + 3]});
    }
    if (__builtin_expect(SLOW, 0)) {
      if (s + 1 == swk) {
        LAS const char* st = lds + 4 * SLOT + wq * 8192 + lane * 16;
#pragma unroll
        for (int i = 0; i < 8; ++i) qf[i] = *(LAS const bf16x8*)(st + i * 1024);
        mref = 0.f; negm = f32x16{};
      }
    }
    asm volatile("" : "+v"(pf[0]), "+v"(pf[1]), "+v"(pf[2]), "+v"(pf[3]), "+v"(lsum), "+v"(negm));
  };
  auto warm_k3 = [&]() __attribute__((always_inline)) {
#pragma unroll
    for (int i = 0; i < 3; ++i) {
      int sec = (wq * 3 + i) * 64 + lane; sec = sec < 736 ? sec : 735;
      const int kr = 1472 * ((b & 7) >> 1) + 46 * (b >> 3) + (sec >> 4);
      const char* src = (kr < 4096 ? (const char*)Wo + (size_t)kr * 1024 : (const char*)W2 + (size_t)(kr - 4096) * 1024) + (sec & 15) * 64;
      touch64(src, lds0 + 8 * SLOT + wq * 256);
    }
  };
#define VBAR(N) do { asm volatile("s_waitcnt vmcnt(" #N ")\n\ts_barrier" ::: "memory"); SCHED; } while (0)
  const unsigned spec = grp ? 0x18001u : (0x0001u | (7u << (sw - 2)));
  LAS char* pb = lds + (wq == 0 ? 9 : (wq == 1 ? 1 : 2 + wq)) * SLOT + lane * 16;
  LAS char* mlp = lds + 2 * SLOT + wq * 512 + lane * 8;
  STAMP(1, 3);
  if (grp == 1) LBAR();
  MPRE(0, 0); SCHED;
  MSLOT(0, 0, 16, false);
  WBAR(0);
  for (int s = 0; s < 16; ++s) {
    sslot(s, (spec >> s) & 1u);
    MPRE(s + 1, 0);
    if (__builtin_expect(s == swk, 0)) VBAR(12); else VBAR(4);
    MSLOT(s + 1, 0, 32, true);
    WBAR(4);
  }
  STAMP(1, 37);
  sslot(16, true);
  WBAR(0);
  if (grp == 1) warm_k3();
  MPRE(17, 16); SCHED;
  MSLOT(17, 16, 32, false);
  STAMP(1, 40);
  if (grp == 0) {
#pragma unroll
    for (int d = 0; d < 4; ++d)
#pragma unroll
      for (int i = 0; i < 4; ++i) *(LAS f32x4*)(pb + (d * 4 + i) * 1024) = (f32x4){o[d][4 * i], o[d][4 * i + 1], o[d][4 * i + 2], o[d][4 * i + 3]};
    *(LAS f32x2*)mlp = (f32x2){mref, lsum};
    LBAR();
  } else {
    const f32x2 ml = *(LAS const f32x2*)mlp;
    const float mn = fmaxf(ml[0], mref), fa = __builtin_amdgcn_exp2f(ml[0] - mn), fb = __builtin_amdgcn_exp2f(mref - mn);
    float l2 = lsum * fb + ml[1] * fa;
    auto rr = __builtin_amdgcn_permlane32_swap(__float_as_uint(l2), __float_as_uint(l2), false, false);
    const float inv = 1.0f / (__uint_as_float(rr[0]) + __uint_as_float(rr[1]));
    const float ga = fa * inv, gb = fb * inv;
#pragma unroll
    for (int d = 0; d < 4; ++d)
#pragma unroll
      for (int i = 0; i < 4; ++i) {
        const f32x4 pa = *(LAS const f32x4*)(pb + (d * 4 + i) * 1024);
#pragma unroll
        for (int e = 0; e < 4; ++e) o[d][4 * i + e] = o[d][4 * i + e] * gb + pa[e] * ga;
      }
    store_ctx(15 - x, 1.0f);
  }
#undef FRD
#undef MSLOT
#undef MPRE
#undef MBODY
#undef KPIECE
#undef VPIECE
  STAMP(1, 41); STAMPR(1, 63);
}

constexpr int K3_LDS = 4 * 32768 + 256;
__global__ __launch_bounds__(512, 2) void k3_out(const u16* __restrict__ Abuf, const u16* __restrict__ Wo, const u16* __restrict__ W2, const u16* __restrict__ x,
                                                 const u16* __restrict__ b2, float* __restrict__ slabs, u16* __restrict__ out) {
  extern __shared__ __attribute__((aligned(16))) char smem[];
  LAS char* lds = (LAS char*)smem;
  const unsigned lds0 = (unsigned)(uintptr_t)lds;
  const int tid = threadIdx.x, lane = tid & 63, wid = rfl(tid >> 6), wr = wid >> 2, wc = wid & 3, fr = lane & 15, fq = lane >> 4;
  const int b = blockIdx.x, xcd = b & 7, idx = b >> 3, sp = xcd >> 1, rt = (xcd & 1) * 8 + (idx >> 2), ct = idx & 3;
  const int kt0 = sp * 23;
  const char* Ab = (const char*)Abuf + (size_t)(rt * NKT + kt0) * 16384;
  const unsigned voffa = (unsigned)(wid * 2048 + lane * 16);
  const unsigned voffb = (unsigned)((8 * wid + (lane >> 3)) * DM + 128 * ct + 16 * (((lane >> 1) & 3) ^ (((lane >> 4) & 1) | ((wid & 1) << 1))) + 8 * (lane & 1)) * 2u;
#define ROT_C 6
#define ROT_R 3
#define PFD 0
  const int rot = (ct * ROT_C + (idx >> 2) * ROT_R) % 23;
  auto stage = [&](int it) __attribute__((always_inline)) {
    int kr = it + rot; kr = kr >= 23 ? kr - 23 : kr;
    const int kt = kt0 + kr;
    const char* wb = kt < 64 ? (const char*)Wo + (size_t)kt * 64 * DM * 2 : (const char*)W2 + (size_t)(kt - 64) * 64 * DM * 2;
    const unsigned d = lds0 + (it & 3) * 32768 + wid * 2048;
    dma2k(Ab + (size_t)kr * 16384, voffa, d);
    dma1k(wb, voffb, d + 16384); dma1k(wb + 128, voffb, d + 16384 + 1024);
  };
  const unsigned pfa = (unsigned)(ct * 4096 + lane * 64), pfw = (unsigned)((((idx >> 2) * 8 + ((lane & 31) >> 2)) * DM + 128 * ct) * 2 + (lane & 3) * 64);
  auto warm = [&](int it, int part) __attribute__((always_inline)) {
    int kr = it + rot; kr = kr >= 23 ? kr - 23 : kr;
    const int kt = kt0 + kr;
    const char* wb = kt < 64 ? (const char*)Wo + (size_t)kt * 64 * DM * 2 : (const char*)W2 + (size_t)(kt - 64) * 64 * DM * 2;
    const char* src = part == 0 ? Ab + (size_t)kr * 16384 + pfa : wb + pfw;
    touch64(src, lds0 + 4 * 32768);
  };
  const int aoff = fq * 2048 + (wr * 64 + fr) * 16;
  const int boff0 = 16384 + fq * 2048 + (wc >> 1) * 1024 + ((lane >> 2) & 3) * 128 + (((2 * (wc & 1)) ^ (((lane >> 3) & 1) | ((fq & 1) << 1))) * 32) + 8 * (lane & 3), boff1 = boff0 ^ 32;
  f32x4 acc[4][2];
#pragma unroll
  for (int m = 0; m < 4; ++m) { acc[m][0] = (f32x4){0.f, 0.f, 0.f, 0.f}; acc[m][1] = (f32x4){0.f, 0.f, 0.f, 0.f}; }
  bf16x8 a0[4][2], b0[2][2], a1[4][2], b1[2][2];
#define K3C 1
#define K3D 1
#define K3_RDB(bf, it_, n, k) bf[n][k] = vtr2(lds + ((it_) & 3) * 32768 + ((n) ? boff1 : boff0) + (k) * 8192, lds + ((it_) & 3) * 32768 + ((n) ? boff1 : boff0) + (k) * 8192 + 512)
#define K3_RDA(af, it_, m, k) af[m][k] = *(LAS const bf16x8*)(lds + ((it_) & 3) * 32768 + aoff + (k) * 8192 + (m) * 256)
#define K3_STEP(it_, ac, bc, an, bn, RD, ST) do { \
    if (PFD > 0 && (it_) + 4 + PFD < 23 && (wid >> 1) == ((it_) & 3)) warm((it_) + 4 + PFD, wid & 1); \
    if (K3D && (ST) && wr == 0) stage((it_) + 4); \
    _Pragma("unroll") for (int k = 0; k < 2; ++k) { _Pragma("unroll") for (int m = 0; m < 4; ++m) { \
      if (K3C) { if (RD) { if (m < 2) { if (k == 0) { K3_RDB(bn, (it_) + 1, m, 0); } else { K3_RDB(bn, (it_) + 1, m, 1); } } K3_RDA(an, (it_) + 1, m, k); } \
      acc[m][0] = MFMA16(bc[0][k], ac[m][k], acc[m][0]); acc[m][1] = MFMA16(bc[1][k], ac[m][k], acc[m][1]); } \
      SCHED; } \
      if (K3D && k == 0 && (ST) && wr == 1) { stage((it_) + 4); SCHED; } } } while (0)
  STAMPR(2, 62); STAMP(2, 0);
  stage(0); stage(1); stage(2); stage(3);
  if (PFD > 0) { for (int j = wid; j < 2 * PFD; j += 8) if (4 + (j >> 1) < 23) warm(4 + (j >> 1), j & 1); }
  WBAR(12);
#pragma unroll
  for (int n = 0; n < 2; ++n) { K3_RDB(b0, 0, n, 0); K3_RDB(b0, 0, n, 1); }
#pragma unroll
  for (int m = 0; m < 4; ++m) { K3_RDA(a0, 0, m, 0); K3_RDA(a0, 0, m, 1); }
  STAMP(2, 1);
#define K3W(n) WBAR(n)
  for (int it = 0; it < 18; it += 2) {
    K3W(8);
    K3_STEP(it, a0, b0, a1, b1, true, true);
    K3W(8);
    K3_STEP(it + 1, a1, b1, a0, b0, true, true);
  }
  WBAR(8); K3_STEP(18, a0, b0, a1, b1, true, true);
  WBAR(8); K3_STEP(19, a1, b1, a0, b0, true, false);
  WBAR(4); K3_STEP(20, a0, b0, a1, b1, true, false);
  WBAR(0); K3_STEP(21, a1, b1, a0, b0, true, false);
  K3_STEP(22, a0, b0, a1, b1, false, false);
#undef K3_RDA
#undef K3_RDB
#undef K3_STEP
  STAMP(2, 30);
  const int tile = rt * 4 + ct;
  const __amdgpu_buffer_rsrc_t rs = __builtin_amdgcn_make_buffer_rsrc((void*)(slabs + (size_t)tile * 12 * 4096), (short)0, 12 * 16384, 0x00020000);
  const int sl = wid * 2048 + lane * 16;
  f32x4 own[2] = {acc[0][0], acc[0][1]};
#pragma unroll
  for (int q = 0; q < 4; ++q) {
    if (q != sp) {
      const int slot = q * 3 + (sp > q ? sp - 1 : sp);
#pragma unroll
      for (int n = 0; n < 2; ++n) {
        u32x4 w = __builtin_bit_cast(u32x4, acc[q][n]);
#pragma unroll
        for (int e = 0; e < 4; ++e) w[e] = w[e] == SENT ? 0x7fc00000u : w[e];
        __builtin_amdgcn_raw_buffer_store_b128(w, rs, slot * 16384 + sl + n * 1024, 0, 17);
      }
    } else { own[0] = acc[q][0]; own[1] = acc[q][1]; }
  }
  STAMP(2, 31); STAMPR(2, 63);
  {
    const size_t roff = (size_t)(rt * 128 + 64 * wr + 16 * sp + fr) * DM + ct * 128 + 32 * wc + 4 * fq;
    u32x2 xx[2], bb[2];
#pragma unroll
    for (int n = 0; n < 2; ++n) { xx[n] = *(const u32x2*)(x + roff + 16 * n); bb[n] = *(const u32x2*)(b2 + ct * 128 + 32 * wc + 4 * fq + 16 * n); }
    u32x4 t[3][2];
    for (int tries = 0; tries < (1 << 14); ++tries) {
      bool ok = true;
      asm volatile("" ::: "memory");
#pragma unroll
      for (int o = 0; o < 3; ++o)
#pragma unroll
        for (int n = 0; n < 2; ++n) t[o][n] = __builtin_amdgcn_raw_buffer_load_b128(rs, (sp * 3 + o) * 16384 + sl + n * 1024, 0, 17);
#pragma unroll
      for (int o = 0; o < 3; ++o)
#pragma unroll
        for (int n = 0; n < 2; ++n)
#pragma unroll
          for (int e = 0; e < 4; ++e) ok = ok && (t[o][n][e] != SENT);
      if (__all(ok)) break;
      __builtin_amdgcn_s_sleep(8);
    }
#pragma unroll
    for (int n = 0; n < 2; ++n) {
      const f32x4 t0 = __builtin_bit_cast(f32x4, t[0][n]), t1 = __builtin_bit_cast(f32x4, t[1][n]), t2 = __builtin_bit_cast(f32x4, t[2][n]);
      const f32x4 p0 = sp == 0 ? own[n] : t0, p1 = sp == 1 ? own[n] : (sp < 1 ? t0 : t1), p2 = sp == 2 ? own[n] : (sp < 2 ? t1 : t2), p3 = sp == 3 ? own[n] : t2;
      const f32x4 a = ((p0 + p1) + p2) + p3;
      u32x2 w = {cvtpk(a[0] + bflo(xx[n][0]) + bflo(bb[n][0]), a[1] + bfhi(xx[n][0]) + bfhi(bb[n][0])), cvtpk(a[2] + bflo(xx[n][1]) + bflo(bb[n][1]), a[3] + bfhi(xx[n][1]) + bfhi(bb[n][1]))};
      *(u32x2*)(out + roff + 16 * n) = w;
    }
  }
  STAMP(2, 32); STAMPR(2, 61);
}

extern "C" void kernel_launch(void* const* d_in, const int* in_sizes, int n_in, void* d_out, int out_size, void* d_ws, size_t ws_size,
                              hipStream_t stream) {
  const u16* x = (const u16*)d_in[0];
  const u16* ckv = (const u16*)d_in[1];
  const u16* wq = (const u16*)d_in[2];
  const u16* wuk = (const u16*)d_in[3];
  const u16* wuv = (const u16*)d_in[4];
  const u16* wo = (const u16*)d_in[5];
  const u16* w1 = (const u16*)d_in[7];
  const u16* b1 = (const u16*)d_in[8];
  const u16* w2 = (const u16*)d_in[9];
  const u16* b2 = (const u16*)d_in[10];
  char* ws = (char*)d_ws;
  u16* Kws = (u16*)(ws + OFF_K); u16* Vws = (u16*)(ws + OFF_V); u16* xn_t = (u16*)(ws + OFF_XN); u16* Abuf = (u16*)(ws + OFF_A);
  float* slabs = (float*)(ws + OFF_SLAB);
  (void)hipFuncSetAttribute((const void*)k1_kv, hipFuncAttributeMaxDynamicSharedMemorySize, K1_LDS);
  (void)hipFuncSetAttribute((const void*)k2_attn, hipFuncAttributeMaxDynamicSharedMemorySize, K2_LDS);
  (void)hipFuncSetAttribute((const void*)k3_out, hipFuncAttributeMaxDynamicSharedMemorySize, K3_LDS);
  k1_kv<<<256, 512, K1_LDS, stream>>>(x, ckv, wuk, wuv, Kws, Vws, xn_t, wq, w1);
  k2_attn<<<256, 512, K2_LDS, stream>>>(xn_t, wq, w1, b1, Kws, Vws, Abuf, slabs, wo, w2);
  k3_out<<<256, 512, K3_LDS, stream>>>(Abuf, wo, w2, x, b2, slabs, (u16*)d_out);
}
```

```cpp
#include <hip/hip_runtime.h>
#include <hip/hip_bf16.h>
#include <stdint.h>
#include <type_traits>

typedef unsigned short u16;
typedef short bf16x8 __attribute__((ext_vector_type(8)));
typedef short s16x4 __attribute__((ext_vector_type(4)));
typedef short v4i16_t __attribute__((ext_vector_type(4)));
typedef float f32x2 __attribute__((ext_vector_type(2)));
typedef float f32x4 __attribute__((ext_vector_type(4)));
typedef float f32x16 __attribute__((ext_vector_type(16)));
typedef unsigned u32x4 __attribute__((ext_vector_type(4)));
typedef unsigned u32x2 __attribute__((ext_vector_type(2)));
typedef __bf16 bf16x2_t __attribute__((ext_vector_type(2)));
#define LAS __attribute__((address_space(3)))
#define DI __device__ __forceinline__

constexpr int SEQ = 2048, DM = 512, NH = 32, HD = 128, FF = 1792, KTOT = NH * HD + FF, NKT = KTOT / 64;
constexpr size_t MiB = 1024 * 1024;
#define WT_AUX 0
constexpr unsigned SENT = 0xffffffffu;
constexpr size_t OFF_K = 0, OFF_V = 16 * MiB, OFF_XN = 32 * MiB, OFF_A = 34 * MiB, OFF_SLAB = 58 * MiB, OFF_CNT = 74 * MiB;

DI unsigned cvtpk(float lo, float hi) { f32x2 v = {lo, hi}; bf16x2_t b = __builtin_convertvector(v, bf16x2_t); return __builtin_bit_cast(unsigned, b); }
DI float rbf(float x) { return __uint_as_float(cvtpk(x, x) & 0xffff0000u); }
DI float bflo(unsigned w) { return __uint_as_float(w << 16); }
DI float bfhi(unsigned w) { return __uint_as_float(w & 0xffff0000u); }
DI s16x4 vtr(LAS const char* p) { return __builtin_bit_cast(s16x4, __builtin_amdgcn_ds_read_tr16_b64_v4i16((LAS v4i16_t*)p)); }
DI bf16x8 vtr2(LAS const char* p0, LAS const char* p1) { s16x4 a = vtr(p0), b = vtr(p1); return __builtin_shufflevector(a, b, 0, 1, 2, 3, 4, 5, 6, 7); }
DI float max3f(float a, float b, float c) { float r; asm("v_max3_f32 %0, %1, %2, %3" : "=v"(r) : "v"(a), "v"(b), "v"(c)); return r; }
DI float max2f(float a, float b) { float r; asm("v_max_f32_e32 %0, %1, %2" : "=v"(r) : "v"(a), "v"(b)); return r; }
DI float fadd_s(float a, float b) { float r; asm("v_add_f32_e32 %0, %1, %2" : "=v"(r) : "v"(a), "v"(b)); return r; }
DI int rfl(int x) { return __builtin_amdgcn_readfirstlane(x); }
#define SCHED __builtin_amdgcn_sched_barrier(0)
#define WAIT_V(n) asm volatile("s_waitcnt vmcnt(" #n ")" ::: "memory")
#define WAIT_L(n) asm volatile("s_waitcnt lgkmcnt(" #n ")" ::: "memory")
#define BAR __builtin_amdgcn_s_barrier()
#define EXP2(x) __builtin_amdgcn_exp2f(x)
#define MFMA32(a, b, c) __builtin_amdgcn_mfma_f32_32x32x16_bf16((a), (b), (c), 0, 0, 0)
#define MFMA16(a, b, c) __builtin_amdgcn_mfma_f32_16x16x32_bf16((a), (b), (c), 0, 0, 0)
#define STAMP(kid, i) do {} while (0)
#define STAMPR(kid, i) do {} while (0)

DI void dma1k(const void* sbase, unsigned voff, unsigned ldsdst) {
  unsigned sv;
  asm volatile("s_mov_b32 %0, m0\n\ts_mov_b32 m0, %3\n\ts_nop 0\n\t"
               "global_load_lds_dwordx4 %1, %2\n\t"
               "s_mov_b32 m0, %0" : "=&s"(sv) : "v"(voff), "s"(sbase), "s"(ldsdst) : "memory");
}
DI void touch64(const void* p, unsigned ldsdump) {
  unsigned sv;
  asm volatile("s_mov_b32 %0, m0\n\ts_mov_b32 m0, %2\n\ts_nop 0\n\t"
               "global_load_lds_dword %1, off\n\t"
               "s_mov_b32 m0, %0" : "=&s"(sv) : "v"(p), "s"(ldsdump) : "memory");
}
DI void dma2k(const void* sbase, unsigned voff, unsigned ldsdst) {
  unsigned sv;
  asm volatile("s_mov_b32 %0, m0\n\ts_mov_b32 m0, %3\n\ts_nop 0\n\t"
               "global_load_lds_dwordx4 %1, %2\n\t"
               "global_load_lds_dwordx4 %1, %2 offset:1024\n\t"
               "s_mov_b32 m0, %0" : "=&s"(sv) : "v"(voff), "s"(sbase), "s"(ldsdst) : "memory");
}
DI void dma4k(const void* sbase, unsigned voff, unsigned ldsdst) {
  unsigned sv;
  asm volatile("s_mov_b32 %0, m0\n\ts_mov_b32 m0, %3\n\ts_nop 0\n\t"
               "global_load_lds_dwordx4 %1, %2\n\t"
               "global_load_lds_dwordx4 %1, %2 offset:1024\n\t"
               "global_load_lds_dwordx4 %1, %2 offset:2048\n\t"
               "global_load_lds_dwordx4 %1, %2 offset:3072\n\t"
               "s_mov_b32 m0, %0" : "=&s"(sv) : "v"(voff), "s"(sbase), "s"(ldsdst) : "memory");
}

constexpr int K1_HTB = 128 * 64 * 2, K1_LDS = 8 * K1_HTB;
DI int lds_byte(int r, int c) { const int st = (r >> 4) * 2 + (c >> 5), rr = r & 15, cc = c & 31, ob = rr * 64 + cc * 2; return st * 1024 + (ob ^ (((ob >> 9) & 1) << 5)); }
DI void stage_rc(int b, int& R, int& C) { const int st = b / 1024, sb = b % 1024, swz = sb ^ (((sb >> 9) & 1) << 5); R = (st >> 1) * 16 + swz / 64; C = (st & 1) * 32 + (swz % 64) / 2; }

template <bool ISV>
DI void k1_body(const u16* __restrict__ A, const u16* __restrict__ W, u16* __restrict__ out, int pm, int pn, LAS char* lds,
                const u16* __restrict__ xrow, u16* __restrict__ xn_t, int row, const char* __restrict__ warm_src) {
  const int tid = threadIdx.x, wid = rfl(tid >> 6), lane = tid & 63, wr = wid >> 2, wc = wid & 3, fr = lane & 15, fq = lane >> 4;
  STAMPR(0, 62); STAMP(0, 0);
  u32x4 xv;
  asm volatile("global_load_dwordx4 %0, %1, off" : "=v"(xv) : "v"(xrow + lane * 8) : "memory");
  unsigned voffA[2], voffB[2];
#pragma unroll
  for (int i = 0; i < 2; ++i) {
    int R, C; stage_rc(tid * 16 + i * 8192, R, C); voffA[i] = (unsigned)(R * DM + C) * 2u;
    const int b = tid * 16 + i * 8192, kb = b >> 11, nh = (b >> 10) & 1, kr = (b >> 7) & 7, c16 = (b >> 4) & 7;
    const int slot = (c16 >> 1) ^ (((kr >> 1) & 1) | ((kb & 1) << 1));
    voffB[i] = (unsigned)((8 * kb + kr) * 4096 + 64 * nh + 16 * slot + 8 * (c16 & 1)) * 2u;
  }
  const unsigned ldsw = (unsigned)wid * 1024u;
  const int aoff = lds_byte(wr * 64 + fr, fq * 8);
  const int boff0 = fq * 2048 + (wc >> 1) * 1024 + ((lane >> 2) & 3) * 128 + (((2 * (wc & 1)) ^ (((lane >> 3) & 1) | ((fq & 1) << 1))) * 32) + 8 * (lane & 3), boff1 = boff0 ^ 32;
  const char* cA = (const char*)A + (size_t)pm * 256 * DM * 2;
  const char* cB = (const char*)W + (size_t)pn * 256 * 2;
  constexpr size_t AK = 64 * 2, AH = (size_t)128 * DM * 2, BK_ = (size_t)64 * 4096 * 2, BH = 128 * 2;
#define SA(b, h) (((b) * 2 + (h)) * K1_HTB)
#define SB(b, h) ((4 + (b) * 2 + (h)) * K1_HTB)
#define STAGE(bufoff, gbase, voff) do { _Pragma("unroll") for (int _i = 0; _i < 2; ++_i) \
    __builtin_amdgcn_global_load_lds((const unsigned*)((const char*)(gbase) + (voff)[_i]), (LAS unsigned*)(lds + (bufoff) + ldsw + _i * 8192), 16, 0, 0); } while (0)
#define STA(b, h, kt) STAGE(SA(b, h), cA + (h) * AH + (kt) * AK, voffA)
#define STB(b, h, kt) STAGE(SB(b, h), cB + (h) * BH + (kt) * BK_, voffB)
#define LDA(dst, b, h) do { _Pragma("unroll") for (int m = 0; m < 4; ++m) _Pragma("unroll") for (int k = 0; k < 2; ++k) dst[m][k] = *(const LAS bf16x8*)(lds + SA(b, h) + aoff + m * 2048 + k * 1024); } while (0)
#define LDB(dst, b, h) do { _Pragma("unroll") for (int n = 0; n < 2; ++n) _Pragma("unroll") for (int k = 0; k < 2; ++k) \
    dst[n][k] = vtr2(lds + SB(b, h) + (n ? boff1 : boff0) + k * 8192, lds + SB(b, h) + (n ? boff1 : boff0) + k * 8192 + 512); } while (0)
#define MMA(ai, bj, At, Bt) do { __builtin_amdgcn_s_setprio(1); _Pragma("unroll") for (int m = 0; m < 4; ++m) _Pragma("unroll") for (int n = 0; n < 2; ++n) _Pragma("unroll") for (int k = 0; k < 2; ++k) \
    acc[ai][bj][m][n] = ISV ? MFMA16(At[m][k], Bt[n][k], acc[ai][bj][m][n]) : MFMA16(Bt[n][k], At[m][k], acc[ai][bj][m][n]); __builtin_amdgcn_s_setprio(0); } while (0)
  f32x4 acc[2][2][4][2];
#pragma unroll
  for (int a = 0; a < 2; ++a)
#pragma unroll
    for (int b = 0; b < 2; ++b)
#pragma unroll
      for (int m = 0; m < 4; ++m)
#pragma unroll
        for (int n = 0; n < 2; ++n) acc[a][b][m][n] = (f32x4){0.f, 0.f, 0.f, 0.f};
  bf16x8 At[4][2], B0[2][2], B1[2][2];
  constexpr int nt = DM / 64;
  STB(0, 0, 0); STA(0, 0, 0); STB(0, 1, 0); STA(0, 1, 0);
  if (wr == 1) BAR;
  asm volatile("s_waitcnt vmcnt(4)" : "+v"(xv) :: "memory");
  BAR;
  {
    float v[8];
#pragma unroll
    for (int i = 0; i < 4; ++i) { v[2 * i] = bflo(xv[i]); v[2 * i + 1] = bfhi(xv[i]); }
    float s = 0.f;
#pragma unroll
    for (int i = 0; i < 8; ++i) s += v[i];
#pragma unroll
    for (int o = 32; o >= 1; o >>= 1) s += __shfl_xor(s, o);
    const float mean = s * (1.0f / DM);
    float q = 0.f;
#pragma unroll
    for (int i = 0; i < 8; ++i) { const float d = v[i] - mean; q += d * d; }
#pragma unroll
    for (int o = 32; o >= 1; o >>= 1) q += __shfl_xor(q, o);
    const float mb = rbf(mean), vb = rbf(q * (1.0f / DM));
    const float den = rbf(sqrtf(rbf(vb + 1e-5f)));
    u32x4 o4;
#pragma unroll
    for (int i = 0; i < 4; ++i) o4[i] = cvtpk(rbf(v[2 * i] - mb) / den, rbf(v[2 * i + 1] - mb) / den);
    *(u32x4*)(xn_t + ((size_t)((row >> 4) * 16 + (lane >> 2)) * 64 + (lane & 3) * 16 + (row & 15)) * 8) = o4;
  }
  STB(1, 0, 1); STA(1, 0, 1); STB(1, 1, 1);
  WAIT_V(6); BAR;
  STAMP(0, 1);
  for (int t = 0; t < nt - 2; t += 2) {
    LDB(B0, 0, 0); SCHED; LDA(At, 0, 0); STA(1, 1, t + 1);
    WAIT_L(8); BAR; WAIT_L(0); MMA(0, 0, At, B0); BAR; SCHED;
    LDB(B1, 0, 1); STB(0, 0, t + 2);
    BAR; WAIT_L(0); MMA(0, 1, At, B1); BAR;
    LDA(At, 0, 1); STA(0, 0, t + 2);
    BAR; WAIT_L(0); MMA(1, 0, At, B0); BAR; SCHED;
    STB(0, 1, t + 2);
    WAIT_V(6); BAR; MMA(1, 1, At, B1); BAR;
    LDB(B0, 1, 0); SCHED; LDA(At, 1, 0); STA(0, 1, t + 2);
    WAIT_L(8); BAR; WAIT_L(0); MMA(0, 0, At, B0); BAR; SCHED;
    LDB(B1, 1, 1); STB(1, 0, t + 3);
    BAR; WAIT_L(0); MMA(0, 1, At, B1); BAR;
    LDA(At, 1, 1); STA(1, 0, t + 3);
    BAR; WAIT_L(0); MMA(1, 0, At, B0); BAR; SCHED;
    STB(1, 1, t + 3);
    WAIT_V(6); BAR; MMA(1, 1, At, B1); BAR;
  }
  { LDB(B0, 0, 0); WAIT_V(0); LDA(At, 0, 0); STA(1, 1, nt - 1);
    BAR; WAIT_L(0); MMA(0, 0, At, B0); BAR;
    LDB(B1, 0, 1); BAR; WAIT_L(0); MMA(0, 1, At, B1); BAR;
    LDA(At, 0, 1); WAIT_V(4); BAR; WAIT_L(0); MMA(1, 0, At, B0); MMA(1, 1, At, B1); BAR; }
  { LDB(B0, 1, 0); LDA(At, 1, 0); WAIT_V(2); BAR; WAIT_L(0); MMA(0, 0, At, B0); BAR;
    LDB(B1, 1, 1); WAIT_V(0); BAR; WAIT_L(0); MMA(0, 1, At, B1); BAR;
    LDA(At, 1, 1); BAR; WAIT_L(0); MMA(1, 0, At, B0); MMA(1, 1, At, B1); BAR; }
  if (wr == 0) BAR;
  STAMP(0, 2);
#pragma unroll
  for (int ai = 0; ai < 2; ++ai)
#pragma unroll
    for (int bj = 0; bj < 2; ++bj) {
      const int head = 2 * pn + bj, tile = 4 * pm + 2 * ai + wr;
      const __amdgpu_buffer_rsrc_t trs = __builtin_amdgcn_make_buffer_rsrc((void*)(out + (size_t)(head * 32 + tile) * 8192), (short)0, 16384, 0x00020000);
      if (!ISV) {
#pragma unroll
        for (int m = 0; m < 4; ++m) {
          const f32x4 a0 = acc[ai][bj][m][0], a1 = acc[ai][bj][m][1];
          u32x4 w = {cvtpk(a0[0], a0[1]), cvtpk(a0[2], a0[3]), cvtpk(a1[0], a1[1]), cvtpk(a1[2], a1[3])};
          __builtin_amdgcn_raw_buffer_store_b128(w, trs, ((4 * wc + fq) * 64 + 16 * m + fr) * 16, 0, WT_AUX);
        }
      } else {
#pragma unroll
        for (int mp = 0; mp < 2; ++mp)
#pragma unroll
          for (int n = 0; n < 2; ++n) {
            const f32x4 a0 = acc[ai][bj][2 * mp][n], a1 = acc[ai][bj][2 * mp + 1][n];
            u32x4 w = {cvtpk(a0[0], a0[1]), cvtpk(a0[2], a0[3]), cvtpk(a1[0], a1[1]), cvtpk(a1[2], a1[3])};
            __builtin_amdgcn_raw_buffer_store_b128(w, trs, ((4 * mp + fq) * 128 + 32 * wc + 16 * n + fr) * 16, 0, WT_AUX);
          }
      }
    }
  STAMP(0, 3); STAMPR(0, 63);
#undef SA
#undef SB
#undef STAGE
#undef STA
#undef STB
#undef LDA
#undef LDB
#undef MMA
}

__global__ __launch_bounds__(512, 2) void k1_kv(const u16* __restrict__ x, const u16* __restrict__ ckv, const u16* __restrict__ wuk, const u16* __restrict__ wuv,
                                                u16* __restrict__ Kws, u16* __restrict__ Vws, u16* __restrict__ xn_t, const u16* __restrict__ wq, const u16* __restrict__ w1) {
  extern __shared__ __attribute__((aligned(16))) char smem[];
  const int b = blockIdx.x, xcd = b & 7, idx = b >> 3;
  const int pnn = ((idx >> 4) << 4) + xcd * 2 + ((idx >> 3) & 1), pm = idx & 7;
  const int row = b * 8 + rfl(threadIdx.x >> 6);
  const int wv = rfl(threadIdx.x >> 6), ln = threadIdx.x & 63;
  const char* warm = nullptr;
  if (wv < 4) warm = (const char*)wq + (size_t)(16 * idx + 4 * wv + (ln >> 4)) * 8192 + xcd * 1024 + (ln & 15) * 64;
  else if (wv < 6 && xcd < 7) warm = (const char*)w1 + (size_t)(16 * idx + 8 * (wv - 4) + (ln >> 3)) * (FF * 2) + xcd * 512 + (ln & 7) * 64;
  if (pnn < 16) k1_body<false>(ckv, wuk, Kws, pm, pnn, (LAS char*)smem, x + (size_t)row * DM, xn_t, row, warm);
  else          k1_body<true>(ckv, wuv, Vws, pm, pnn - 16, (LAS char*)smem, x + (size_t)row * DM, xn_t, row, warm);
}

#define LSTAMP(k) do {} while (0)
constexpr int SLOT = 16384, K2_LDS = 10 * SLOT;
constexpr float QSCALE = 0.08838834764831845f * 1.4426950408889634f;
constexpr float THR = 32.f;
#define WBAR(N) do { asm volatile("s_waitcnt vmcnt(" #N ") lgkmcnt(0)\n\ts_barrier" ::: "memory"); SCHED; } while (0)
#define LBAR() do { asm volatile("s_waitcnt lgkmcnt(0)\n\ts_barrier" ::: "memory"); SCHED; } while (0)
#define VBAR(N) do { asm volatile("s_waitcnt vmcnt(" #N ")\n\ts_barrier" ::: "memory"); SCHED; } while (0)
DI float xg_max(float v) { v = fmaxf(v, __shfl_xor(v, 16)); return fmaxf(v, __shfl_xor(v, 32)); }
DI float xg_sum(float v) { v += __shfl_xor(v, 16); return v + __shfl_xor(v, 32); }
DI bf16x8 pack8(const f32x4& a, const f32x4& b, float sc) {
  return __builtin_bit_cast(bf16x8, (u32x4){cvtpk(a[0] * sc, a[1] * sc), cvtpk(a[2] * sc, a[3] * sc), cvtpk(b[0] * sc, b[1] * sc), cvtpk(b[2] * sc, b[3] * sc)});
}

__global__ __launch_bounds__(512, 2) void k2_attn(const u16* __restrict__ xn_t, const u16* __restrict__ Wq, const u16* __restrict__ W1, const u16* __restrict__ b1,
                                                  const u16* __restrict__ Kws, const u16* __restrict__ Vws, u16* __restrict__ Abuf, float* __restrict__ slabs,
                                                  const u16* __restrict__ Wo, const u16* __restrict__ W2) {
  extern __shared__ __attribute__((aligned(16))) char smem[];
  LAS char* lds = (LAS char*)smem;
  const unsigned lds0 = (unsigned)(uintptr_t)lds;
  const int tid = threadIdx.x, lane = tid & 63, wid = rfl(tid >> 6), grp = wid >> 2, wq = wid & 3, c = lane & 15, g = lane >> 4;
  const int b = blockIdx.x, head = (b & 7) * 4 + (b >> 6), x = (b >> 3) & 7;
  const bool doF = head < 28;
  const int fh = doF ? head : 27;
  const int sw = 2 * x + 2;
  const int blk0 = grp ? 15 - x : x;
  const char* Kh = (const char*)Kws + (size_t)head * 32 * SLOT;
  const char* Vh = (const char*)Vws + (size_t)head * 32 * SLOT;
  const unsigned vlin = (unsigned)(wq * 4096 + lane * 16);
  const int tb0 = grp ? 15 - 2 * x : 0;
  const __amdgpu_buffer_rsrc_t arsrc = __builtin_amdgcn_make_buffer_rsrc((void*)Abuf, (short)0, 16 * NKT * 16384, 0x00020000);
  const int cvoff = (32 * wq + c) * 16 + g * 2048;

  bf16x8 qf[4][2];
  STAMPR(1, 62); STAMP(1, 0);
  auto prologue = [&](auto ftag) __attribute__((always_inline)) {
    constexpr bool DOF = decltype(ftag)::value;
    const char* xbase = (const char*)xn_t + (size_t)(blk0 * 8 + 2 * wq) * 16384;
    const unsigned xvo = lane * 16;
    const int dsl = 16 * (((lane >> 1) & 3) ^ (((lane >> 4) & 1) | ((wid & 1) << 1))) + 8 * (lane & 1);
    const unsigned voffq = (unsigned)((8 * wid + (lane >> 3)) * 4096 + head * 128 + dsl) * 2u;
    const unsigned voff1 = (unsigned)((8 * wid + (lane >> 3)) * FF + fh * 64 + dsl) * 2u;
    constexpr int CH = 24576;
    auto stage_w = [&](int ch) __attribute__((always_inline)) {
      const char* q0 = (const char*)Wq + (size_t)ch * 64 * 4096 * 2;
      const char* f0 = (const char*)W1 + (size_t)ch * 64 * FF * 2;
      const unsigned d0 = lds0 + (ch & 3) * CH + wid * 3072;
      dma1k(q0, voffq, d0); dma1k(q0 + 128, voffq, d0 + 1024); dma1k(f0, voff1, d0 + 2048);
    };
    int wb[4];
#pragma unroll
    for (int sl = 0; sl < 4; ++sl) wb[sl] = g * 3072 + ((lane >> 2) & 3) * 128 + ((sl ^ (((lane >> 3) & 1) | ((g & 1) << 1))) * 32) + 8 * (lane & 3);
#define WFRAG(ch, ks, t) vtr2(lds + ((ch) & 3) * CH + wb[(t) & 3] + (ks) * 12288 + ((t) >> 2) * 1024, lds + ((ch) & 3) * CH + wb[(t) & 3] + (ks) * 12288 + ((t) >> 2) * 1024 + 512)
    f32x4 aq[12][2];
#pragma unroll
    for (int t = 0; t < 12; ++t) { aq[t][0] = (f32x4){0.f, 0.f, 0.f, 0.f}; aq[t][1] = (f32x4){0.f, 0.f, 0.f, 0.f}; }
    bf16x8 xr[4][4];
#define XLOAD(ch) asm volatile("global_load_dwordx4 %0, %4, %5\n\tglobal_load_dwordx4 %1, %4, %5 offset:1024\n\tglobal_load_dwordx4 %2, %4, %6\n\tglobal_load_dwordx4 %3, %4, %6 offset:1024" \
      : "=&v"(xr[(ch) & 3][0]), "=&v"(xr[(ch) & 3][1]), "=&v"(xr[(ch) & 3][2]), "=&v"(xr[(ch) & 3][3]) : "v"(xvo), "s"(xbase + (ch) * 2048), "s"(xbase + 16384 + (ch) * 2048) : "memory")
#define CWAIT(ch, N) do { asm volatile("s_waitcnt vmcnt(" #N ") lgkmcnt(0)\n\ts_barrier" : "+v"(xr[(ch) & 3][0]), "+v"(xr[(ch) & 3][1]), "+v"(xr[(ch) & 3][2]), "+v"(xr[(ch) & 3][3]) :: "memory"); SCHED; } while (0)
    constexpr int WPD = 6;
    bf16x8 wa[24 + WPD];
    auto compute = [&](int ch, auto&& act) __attribute__((always_inline)) {
#pragma unroll
      for (int i = 0; i < 24; ++i) {
        const int ks = i / 12, t = i % 12, in = i + WPD, chn = in < 24 ? ch : ch + 1, ksn = (in % 24) / 12, tn = in % 12;
        if ((in < 24 || ch < 7) && (tn < 8 || DOF)) wa[in] = WFRAG(chn, ksn, tn);
        if (t < 8 || DOF) { aq[t][0] = MFMA16(wa[i], xr[ch & 3][ks], aq[t][0]); aq[t][1] = MFMA16(wa[i], xr[ch & 3][2 + ks], aq[t][1]); }
        if (i % 6 == 1) act(i / 6);
        SCHED;
      }
#pragma unroll
      for (int j = 0; j < WPD; ++j) wa[j] = wa[24 + j];
    };
    u32x2 bb[4];
    const u16* b1p = b1 + fh * 64; const int bvo = g * 8;
    XLOAD(0); stage_w(0); XLOAD(1); stage_w(1); XLOAD(2); stage_w(2);
    CWAIT(0, 14); STAMP(1, 1);
#pragma unroll
    for (int j = 0; j < WPD; ++j) wa[j] = WFRAG(0, 0, j);
    CWAIT(1, 7); compute(0, [&](int i) __attribute__((always_inline)) { if (i == 0) XLOAD(3); if (i == 1) stage_w(3); });
    CWAIT(2, 7); compute(1, [&](int i) __attribute__((always_inline)) { if (i == 0) XLOAD(4); if (i == 1) stage_w(4); });
    CWAIT(3, 7); compute(2, [&](int i) __attribute__((always_inline)) { if (i == 0) XLOAD(5); if (i == 1) stage_w(5); });
    CWAIT(4, 7); compute(3, [&](int i) __attribute__((always_inline)) { if (i == 0) XLOAD(6); if (i == 1) stage_w(6); });
    CWAIT(5, 7); compute(4, [&](int i) __attribute__((always_inline)) { if (i == 0) XLOAD(7); if (i == 1) stage_w(7); });
    CWAIT(6, 7);
    compute(5, [&](int i) __attribute__((always_inline)) {
      if (i == 0) asm volatile("global_load_dwordx2 %0, %4, %5\n\tglobal_load_dwordx2 %1, %4, %5 offset:32\n\tglobal_load_dwordx2 %2, %4, %5 offset:64\n\tglobal_load_dwordx2 %3, %4, %5 offset:96"
                               : "=&v"(bb[0]), "=&v"(bb[1]), "=&v"(bb[2]), "=&v"(bb[3]) : "v"(bvo), "s"(b1p) : "memory");
    });
    CWAIT(7, 4);
    compute(6, [&](int i) __attribute__((always_inline)) {
      if (i == 0) dma4k(Kh + (size_t)tb0 * SLOT, vlin, lds0 + (grp ? 6 : 9) * SLOT + wq * 4096);
      if (i == 1) dma4k(Vh + (size_t)tb0 * SLOT, vlin, lds0 + (grp ? 7 : 0) * SLOT + wq * 4096);
      if (i == 2) dma4k(Kh + (size_t)(tb0 + 1) * SLOT, vlin, lds0 + (grp ? 8 : 1) * SLOT + wq * 4096);
    });
    compute(7, [&](int) __attribute__((always_inline)) {});
    STAMP(1, 2);
    asm volatile("s_waitcnt vmcnt(8) lgkmcnt(0)\n\ts_barrier" : "+v"(bb[0]), "+v"(bb[1]), "+v"(bb[2]), "+v"(bb[3]) :: "memory"); SCHED;
#undef XLOAD
#undef CWAIT
#undef WFRAG
#pragma unroll
    for (int u = 0; u < 4; ++u) { qf[u][0] = pack8(aq[2 * u][0], aq[2 * u + 1][0], QSCALE); qf[u][1] = pack8(aq[2 * u][1], aq[2 * u + 1][1], QSCALE); }
    if (grp == 1) {
      LAS char* st = lds + 4 * SLOT + wq * 8192 + lane * 16;
#pragma unroll
      for (int i = 0; i < 8; ++i) *(LAS bf16x8*)(st + i * 1024) = qf[i >> 1][i & 1];
    }
    if constexpr (DOF) {
      const int sbase = (blk0 * NKT + 64 + head) * 16384;
#pragma unroll
      for (int v = 0; v < 2; ++v)
#pragma unroll
        for (int qt = 0; qt < 2; ++qt) {
          u32x4 w;
#pragma unroll
          for (int hf = 0; hf < 2; ++hf) {
            const f32x4 a = aq[8 + 2 * v + hf][qt]; const u32x2 bv = bb[2 * v + hf];
            float t0 = rbf(rbf(a[0]) + bflo(bv[0])), t1 = rbf(rbf(a[1]) + bfhi(bv[0])), t2 = rbf(rbf(a[2]) + bflo(bv[1])), t3 = rbf(rbf(a[3]) + bfhi(bv[1]));
            t0 = t0 > 0.f ? t0 : 0.f; t1 = t1 > 0.f ? t1 : 0.f; t2 = t2 > 0.f ? t2 : 0.f; t3 = t3 > 0.f ? t3 : 0.f;
            w[2 * hf] = cvtpk(t0 * t0, t1 * t1); w[2 * hf + 1] = cvtpk(t2 * t2, t3 * t3);
          }
          __builtin_amdgcn_raw_buffer_store_b128(w, arsrc, cvoff + sbase + v * 8192 + qt * 256, 0, 0);
        }
    }
  };
  if (doF) prologue(std::true_type{}); else prologue(std::false_type{});
  {
    const __amdgpu_buffer_rsrc_t frs = __builtin_amdgcn_make_buffer_rsrc((void*)(slabs + (size_t)b * 12288), (short)0, 49152, 0x00020000);
#pragma unroll
    for (int i = 0; i < 6; ++i) __builtin_amdgcn_raw_buffer_store_b128((u32x4){SENT, SENT, SENT, SENT}, frs, tid * 16 + i * 8192, 0, 17);
  }
  const int klane = g * 1024 + c * 16, vlane = g * 2048 + c * 16;
  const int kslot0 = (grp ? 6 : 9) * SLOT, kslot1 = (grp ? 8 : 1) * SLOT, vslot0 = (grp ? 7 : 0) * SLOT, vslot1 = (grp ? 3 : 2) * SLOT;
  f32x4 o[8][2], sc[4][2], negm[2];
#pragma unroll
  for (int i = 0; i < 8; ++i) { o[i][0] = (f32x4){0.f, 0.f, 0.f, 0.f}; o[i][1] = (f32x4){0.f, 0.f, 0.f, 0.f}; }
  negm[0] = (f32x4){0.f, 0.f, 0.f, 0.f}; negm[1] = (f32x4){0.f, 0.f, 0.f, 0.f};
  bf16x8 pf[2][2];
  float mref[2] = {0.f, 0.f};
  f32x4 lacc[2] = {(f32x4){0.f, 0.f, 0.f, 0.f}, (f32x4){0.f, 0.f, 0.f, 0.f}};
  bf16x8 ones = {0x3f80, 0x3f80, 0x3f80, 0x3f80, 0x3f80, 0x3f80, 0x3f80, 0x3f80};
  asm volatile("" : "+v"(ones));
  auto store_ctx = [&](int blk, float inv0, float inv1) __attribute__((always_inline)) {
    const int sbase = (blk * NKT + 2 * head) * 16384;
#pragma unroll
    for (int v = 0; v < 4; ++v)
#pragma unroll
      for (int qt = 0; qt < 2; ++qt)
        __builtin_amdgcn_raw_buffer_store_b128(__builtin_bit_cast(u32x4, pack8(o[2 * v][qt], o[2 * v + 1][qt], qt ? inv1 : inv0)), arsrc, cvoff + sbase + v * 8192 + qt * 256, 0, 0);
  };
  constexpr int PD = 6;
  bf16x8 fr_[32];
  const int swk = grp ? 99 : sw;
  int ktile = tb0 + 1, vtile = tb0;
  unsigned kvo = 0, vvo = 0;
#define FRD(i) (((i) < 16) ? *(LAS const bf16x8*)(kp + ((i) >> 2) * 4096 + ((i) & 3) * 256) : *(LAS const bf16x8*)(vp + (((i) - 16) >> 3) * 8192 + (((i) - 16) & 7) * 256))
#define SETM0(v) asm volatile("s_mov_b32 m0, %0\n\ts_nop 0" :: "s"(v) : "memory")
#define DMAI(vo, base, off) asm volatile("global_load_lds_dwordx4 %0, %1 offset:" #off :: "v"(vo), "s"(base) : "memory")
#define MBODY(i) do { if (i < 16) { sc[i & 3][0] = MFMA16(fr_[i], qf[i >> 2][0], i < 4 ? negm[0] : sc[i & 3][0]); sc[i & 3][1] = MFMA16(fr_[i], qf[i >> 2][1], i < 4 ? negm[1] : sc[i & 3][1]); } \
      else { o[(i - 16) & 7][0] = MFMA16(fr_[i], pf[(i - 16) >> 3][0], o[(i - 16) & 7][0]); o[(i - 16) & 7][1] = MFMA16(fr_[i], pf[(i - 16) >> 3][1], o[(i - 16) & 7][1]); \
        if (((i - 16) & 7) == 3) lacc[0] = MFMA16(ones, pf[(i - 16) >> 3][0], lacc[0]); if (((i - 16) & 7) == 5) lacc[1] = MFMA16(ones, pf[(i - 16) >> 3][1], lacc[1]); } } while (0)
#define KPIECE(s_, j) do { if ((j) == 0) { ktile = ((s_) + 2 == swk) ? 0 : ktile + 1; kvo = vlin + (unsigned)min(ktile, 31) * SLOT; SETM0(lds0 + (((s_) & 1) ? kslot1 : kslot0) + wq * 4096); DMAI(kvo, Kh, 0); } \
    if ((j) == 1) DMAI(kvo, Kh, 1024); if ((j) == 2) DMAI(kvo, Kh, 2048); if ((j) == 3) DMAI(kvo, Kh, 3072); } while (0)
#define VPIECE(s_, j) do { if ((j) == 0) { SETM0(lds0 + (((s_) & 1) ? vslot1 : vslot0) + wq * 4096); DMAI(vvo, Vh, 0); } \
    if ((j) == 1) DMAI(vvo, Vh, 1024); if ((j) == 2) DMAI(vvo, Vh, 2048); if ((j) == 3) DMAI(vvo, Vh, 3072); } while (0)
#define MPRE(s_, I0) do { \
    LAS const char* kp = lds + (((s_) & 1) ? kslot1 : kslot0) + klane; \
    LAS const char* vp = lds + (((s_) & 1) ? vslot0 : vslot1) + vlane; \
    _Pragma("unroll") for (int i = I0; i < I0 + PD; ++i) fr_[i] = FRD(i); \
    (void)kp; (void)vp; } while (0)
#define MSLOT(s_, I0, I1, DM) do { \
    LAS const char* kp = lds + (((s_) & 1) ? kslot1 : kslot0) + klane; \
    LAS const char* vp = lds + (((s_) & 1) ? vslot0 : vslot1) + vlane; \
    if (DM) { vtile = ((s_) == swk) ? 0 : vtile + 1; vvo = vlin + (unsigned)min(vtile, 31) * SLOT; } \
    _Pragma("unroll") for (int i = I0; i < I1; ++i) { \
      if (i + PD < I1) fr_[i + PD] = FRD(i + PD); \
      MBODY(i); \
      if ((DM) && (i & 3) == 2 && i < 16) VPIECE(s_, i >> 2); \
      SCHED; \
    } (void)kp; (void)vp; } while (0)
#define SCV(i) sc[((i) >> 2) & 3][(i) >> 4][(i) & 3]
  auto sslot = [&](const int s, const bool SLOW) __attribute__((always_inline)) {
    KPIECE(s, 0);
    bool first = false;
    if (__builtin_expect(SLOW, 0)) {
      const bool swst = (s == swk);
      if (swst) {
        store_ctx(x, 1.0f / lacc[0][0], 1.0f / lacc[1][0]);
#pragma unroll
        for (int i = 0; i < 8; ++i) { o[i][0] = (f32x4){0.f, 0.f, 0.f, 0.f}; o[i][1] = (f32x4){0.f, 0.f, 0.f, 0.f}; }
        lacc[0] = (f32x4){0.f, 0.f, 0.f, 0.f}; lacc[1] = (f32x4){0.f, 0.f, 0.f, 0.f};
      }
      first = (s == 0) || swst;
      const int jb = grp ? s - 15 : s - (sw - 2);
      if (jb >= 0 && jb <= 1) {
        const int dq = 32 * wq + c - 64 * jb - 4 * g;
#pragma unroll
        for (int i = 0; i < 32; ++i) if (16 * ((i >> 2) & 3) + (i & 3) > dq + 16 * (i >> 4)) SCV(i) = -__builtin_inff();
      }
    }
    float pm0 = max3f(SCV(0), SCV(1), SCV(2)), pm1 = max3f(SCV(16), SCV(17), SCV(18));
#pragma unroll
    for (int i = 3; i < 15; i += 2) { pm0 = max3f(pm0, SCV(i), SCV(i + 1)); pm1 = max3f(pm1, SCV(16 + i), SCV(17 + i)); }
    pm0 = max2f(pm0, SCV(15)); pm1 = max2f(pm1, SCV(31));
    KPIECE(s, 1);
    if (__builtin_expect(first || __any(max2f(pm0, pm1) > THR), 0)) {
      const float r0 = xg_max(pm0), r1 = xg_max(pm1);
      const float dl[2] = {first ? r0 : fmaxf(r0, 0.f), first ? r1 : fmaxf(r1, 0.f)};
#pragma unroll
      for (int qt = 0; qt < 2; ++qt) {
        mref[qt] += dl[qt];
#pragma unroll
        for (int kt = 0; kt < 4; ++kt) sc[kt][qt] -= dl[qt];
        if (!first) {
          const float al = __builtin_amdgcn_exp2f(-dl[qt]);
          lacc[qt] *= al;
#pragma unroll
          for (int d = 0; d < 8; ++d) o[d][qt] *= al;
        }
        negm[qt] = (f32x4){-mref[qt], -mref[qt], -mref[qt], -mref[qt]};
      }
    }
    {
      unsigned pw[16];
      SCV(0) = __builtin_amdgcn_exp2f(SCV(0)); SCV(1) = __builtin_amdgcn_exp2f(SCV(1)); SCHED;
#pragma unroll
      for (int i = 0; i < 32; i += 2) {
        if (i + 2 < 32) SCV(i + 2) = __builtin_amdgcn_exp2f(SCV(i + 2));
        pw[i >> 1] = cvtpk(SCV(i), SCV(i + 1));
        if (i + 3 < 32) SCV(i + 3) = __builtin_amdgcn_exp2f(SCV(i + 3));
        if (i == 8) KPIECE(s, 2);
        if (i == 20) KPIECE(s, 3);
        SCHED;
      }
#pragma unroll
      for (int k4 = 0; k4 < 4; ++k4) pf[k4 & 1][k4 >> 1] = __builtin_bit_cast(bf16x8, (u32x4){pw[4 * k4], pw[4 * k4 + 1], pw[4 * k4 + 2], pw[4 * k4 + 3]});
    }
    if (__builtin_expect(SLOW, 0)) {
      if (s + 1 == swk) {
        LAS const char* st = lds + 4 * SLOT + wq * 8192 + lane * 16;
#pragma unroll
        for (int i = 0; i < 8; ++i) qf[i >> 1][i & 1] = *(LAS const bf16x8*)(st + i * 1024);
        mref[0] = 0.f; mref[1] = 0.f; negm[0] = (f32x4){0.f, 0.f, 0.f, 0.f}; negm[1] = (f32x4){0.f, 0.f, 0.f, 0.f};
      }
    }
    asm volatile("" : "+v"(pf[0][0]), "+v"(pf[0][1]), "+v"(pf[1][0]), "+v"(pf[1][1]), "+v"(negm[0]), "+v"(negm[1]));
  };
  auto warm_k3 = [&]() __attribute__((always_inline)) {
#pragma unroll
    for (int i = 0; i < 3; ++i) {
      int sec = (wq * 3 + i) * 64 + lane; sec = sec < 736 ? sec : 735;
      const int kr = 1472 * ((b & 7) >> 1) + 46 * (b >> 3) + (sec >> 4);
      const char* src = (kr < 4096 ? (const char*)Wo + (size_t)kr * 1024 : (const char*)W2 + (size_t)(kr - 4096) * 1024) + (sec & 15) * 64;
      touch64(src, lds0 + 8 * SLOT + wq * 256);
    }
  };
  const unsigned spec = grp ? 0x18001u : (0x0001u | (7u << (sw - 2)));
  LAS char* pb = lds + (wq == 0 ? 9 : (wq == 1 ? 1 : 2 + wq)) * SLOT + lane * 16;
  LAS char* mlp = lds + 2 * SLOT + wq * 1024 + lane * 16;
  STAMP(1, 3);
  if (grp == 1) LBAR();
  MPRE(0, 0); SCHED;
  MSLOT(0, 0, 16, false);
  WBAR(0);
  for (int s = 0; s < 16; ++s) {
    LSTAMP(5);
    sslot(s, (spec >> s) & 1u);
    MPRE(s + 1, 0);
    LSTAMP(6);
    if (__builtin_expect(s == swk, 0)) VBAR(12); else VBAR(4);
    LSTAMP(7);
    MSLOT(s + 1, 0, 32, true);
    LSTAMP(8);
    WBAR(4);
  }
  STAMP(1, 37);
  sslot(16, true);
  WBAR(0);
  if (grp == 1) warm_k3();
  MPRE(17, 16); SCHED;
  MSLOT(17, 16, 32, false);
  STAMP(1, 40);
  if (grp == 0) {
#pragma unroll
    for (int i = 0; i < 16; ++i) *(LAS f32x4*)(pb + i * 1024) = o[i >> 1][i & 1];
    *(LAS f32x4*)mlp = (f32x4){mref[0], lacc[0][0], mref[1], lacc[1][0]};
    LBAR();
  } else {
    const f32x4 ml = *(LAS const f32x4*)mlp;
    float ga[2], gb[2];
#pragma unroll
    for (int qt = 0; qt < 2; ++qt) {
      const float mn = fmaxf(ml[2 * qt], mref[qt]), fa = __builtin_amdgcn_exp2f(ml[2 * qt] - mn), fb = __builtin_amdgcn_exp2f(mref[qt] - mn);
      const float inv = 1.0f / (lacc[qt][0] * fb + ml[2 * qt + 1] * fa);
      ga[qt] = fa * inv; gb[qt] = fb * inv;
    }
#pragma unroll
    for (int i = 0; i < 16; ++i) { const f32x4 pa = *(LAS const f32x4*)(pb + i * 1024); o[i >> 1][i & 1] = o[i >> 1][i & 1] * gb[i & 1] + pa * ga[i & 1]; }
    store_ctx(15 - x, 1.0f, 1.0f);
  }
#undef FRD
#undef MSLOT
#undef MPRE
#undef MBODY
#undef KPIECE
#undef VPIECE
#undef SCV
  STAMP(1, 41); STAMPR(1, 63);
}

constexpr int K3_NS = 5, K3_LDS = K3_NS * 32768;
__global__ __launch_bounds__(512, 2) void k3_out(const u16* __restrict__ Abuf, const u16* __restrict__ Wo, const u16* __restrict__ W2, const u16* __restrict__ x,
                                                 const u16* __restrict__ b2, float* __restrict__ slabs, u16* __restrict__ out) {
  extern __shared__ __attribute__((aligned(16))) char smem[];
  LAS char* lds = (LAS char*)smem;
  const unsigned lds0 = (unsigned)(uintptr_t)lds;
  const int tid = threadIdx.x, lane = tid & 63, wid = rfl(tid >> 6), wr = wid >> 2, wc = wid & 3, fr = lane & 15, fq = lane >> 4;
  const int b = blockIdx.x, xcd = b & 7, idx = b >> 3, sp = xcd >> 1, rt = (xcd & 1) * 8 + (idx >> 2), ct = idx & 3;
  const int kt0 = sp * 23;
  const char* Ab = (const char*)Abuf + (size_t)(rt * NKT + kt0) * 16384;
  const unsigned voffa = (unsigned)(wid * 2048 + lane * 16);
  const unsigned voffb = (unsigned)((32 * (wid >> 2) + 16 * (lane >> 5) + 4 * (wid & 3) + ((lane >> 3) & 3)) * DM + 128 * ct + 16 * (((lane >> 1) & 3) ^ (((lane >> 4) & 1) | ((wid & 1) << 1))) + 8 * (lane & 1)) * 2u;
#define ROT_C 0
#define ROT_R 0
#define PFD 0
  const int rot = (ct * ROT_C + (idx >> 2) * ROT_R) % 23;
  auto stage = [&](int it) __attribute__((always_inline)) {
    int kr = it + rot; kr = kr >= 23 ? kr - 23 : kr;
    const int kt = kt0 + kr;
    const char* wb = kt < 64 ? (const char*)Wo + (size_t)kt * 64 * DM * 2 : (const char*)W2 + (size_t)(kt - 64) * 64 * DM * 2;
    const unsigned d = lds0 + (it % K3_NS) * 32768 + wid * 2048;
    dma2k(Ab + (size_t)kr * 16384, voffa, d);
    dma1k(wb, voffb, d + 16384); dma1k(wb + 128, voffb, d + 16384 + 1024);
  };
  const int aoff = fq * 2048 + (wr * 64 + fr) * 16;
  const int boff0 = 16384 + fq * 2048 + (wc >> 1) * 1024 + ((lane >> 2) & 3) * 128 + (((2 * (wc & 1)) ^ (((lane >> 3) & 1) | ((fq & 1) << 1))) * 32) + 8 * (lane & 3), boff1 = boff0 ^ 32;
  f32x4 acc[4][2];
#pragma unroll
  for (int m = 0; m < 4; ++m) { acc[m][0] = (f32x4){0.f, 0.f, 0.f, 0.f}; acc[m][1] = (f32x4){0.f, 0.f, 0.f, 0.f}; }
  bf16x8 a0[4][2], b0[2][2], a1[4][2], b1[2][2];
#define K3C 1
#define K3D 1
#define K3_RDB(bf, it_, n, k) bf[n][k] = vtr2(lds + ((it_) % K3_NS) * 32768 + ((n) ? boff1 : boff0) + (k) * 8192, lds + ((it_) % K3_NS) * 32768 + ((n) ? boff1 : boff0) + (k) * 8192 + 512)
#define K3_RDA(af, it_, m, k) af[m][k] = *(LAS const bf16x8*)(lds + ((it_) % K3_NS) * 32768 + aoff + (k) * 8192 + (m) * 256)
#define K3_STEP(it_, ac, bc, an, bn, RD, ST) do { \
    if (K3D && (ST) && wr == 0) stage((it_) + 5); \
    _Pragma("unroll") for (int k = 0; k < 2; ++k) { _Pragma("unroll") for (int m = 0; m < 4; ++m) { \
      if (K3C) { if (RD) { if (m < 2) { if (k == 0) { K3_RDB(bn, (it_) + 1, m, 0); } else { K3_RDB(bn, (it_) + 1, m, 1); } } K3_RDA(an, (it_) + 1, m, k); } \
      acc[m][0] = MFMA16(bc[0][k], ac[m][k], acc[m][0]); acc[m][1] = MFMA16(bc[1][k], ac[m][k], acc[m][1]); } \
      SCHED; } \
      if (K3D && k == 0 && (ST) && wr == 1) { stage((it_) + 5); SCHED; } } } while (0)
  STAMPR(2, 62); STAMP(2, 0);
  stage(0); stage(1); stage(2); stage(3); stage(4);
  WBAR(16);
#pragma unroll
  for (int n = 0; n < 2; ++n) { K3_RDB(b0, 0, n, 0); K3_RDB(b0, 0, n, 1); }
#pragma unroll
  for (int m = 0; m < 4; ++m) { K3_RDA(a0, 0, m, 0); K3_RDA(a0, 0, m, 1); }
  STAMP(2, 1);
#pragma unroll 1
  for (int it10 = 0; it10 < 10; it10 += 10) {
#pragma unroll
    for (int j = 0; j < 10; j += 2) {
      WBAR(12); K3_STEP(it10 + j, a0, b0, a1, b1, true, true);
      WBAR(12); K3_STEP(it10 + j + 1, a1, b1, a0, b0, true, true);
    }
  }
#pragma unroll
  for (int j = 10; j < 18; j += 2) {
    WBAR(12); K3_STEP(j, a0, b0, a1, b1, true, true);
    WBAR(12); K3_STEP(j + 1, a1, b1, a0, b0, true, true);
  }
  WBAR(12); K3_STEP(18, a0, b0, a1, b1, true, false);
  WBAR(8); K3_STEP(19, a1, b1, a0, b0, true, false);
  WBAR(4); K3_STEP(20, a0, b0, a1, b1, true, false);
  WBAR(0); K3_STEP(21, a1, b1, a0, b0, true, false);
  K3_STEP(22, a0, b0, a1, b1, false, false);
#undef K3_RDA
#undef K3_RDB
#undef K3_STEP
  STAMP(2, 30);
  const int tile = rt * 4 + ct;
  const __amdgpu_buffer_rsrc_t rs = __builtin_amdgcn_make_buffer_rsrc((void*)(slabs + (size_t)tile * 12 * 4096), (short)0, 12 * 16384, 0x00020000);
  const int sl = wid * 2048 + lane * 16;
  f32x4 own[2] = {acc[0][0], acc[0][1]};
#pragma unroll
  for (int q = 0; q < 4; ++q) {
    if (q != sp) {
      const int slot = q * 3 + (sp > q ? sp - 1 : sp);
#pragma unroll
      for (int n = 0; n < 2; ++n) {
        u32x4 w = __builtin_bit_cast(u32x4, acc[q][n]);
#pragma unroll
        for (int e = 0; e < 4; ++e) w[e] = w[e] == SENT ? 0x7fc00000u : w[e];
        __builtin_amdgcn_raw_buffer_store_b128(w, rs, slot * 16384 + sl + n * 1024, 0, 17);
      }
    } else { own[0] = acc[q][0]; own[1] = acc[q][1]; }
  }
  STAMP(2, 31); STAMPR(2, 63);
  {
    const size_t roff = (size_t)(rt * 128 + 64 * wr + 16 * sp + fr) * DM + ct * 128 + 32 * wc + 4 * fq;
    u32x2 xx[2], bb[2];
#pragma unroll
    for (int n = 0; n < 2; ++n) { xx[n] = *(const u32x2*)(x + roff + 16 * n); bb[n] = *(const u32x2*)(b2 + ct * 128 + 32 * wc + 4 * fq + 16 * n); }
    u32x4 t[3][2];
    for (int tries = 0; tries < (1 << 14); ++tries) {
      bool ok = true;
      asm volatile("" ::: "memory");
#pragma unroll
      for (int o = 0; o < 3; ++o)
#pragma unroll
        for (int n = 0; n < 2; ++n) t[o][n] = __builtin_amdgcn_raw_buffer_load_b128(rs, (sp * 3 + o) * 16384 + sl + n * 1024, 0, 17);
#pragma unroll
      for (int o = 0; o < 3; ++o)
#pragma unroll
        for (int n = 0; n < 2; ++n)
#pragma unroll
          for (int e = 0; e < 4; ++e) ok = ok && (t[o][n][e] != SENT);
      if (__all(ok)) break;
      __builtin_amdgcn_s_sleep(8);
    }
#pragma unroll
    for (int n = 0; n < 2; ++n) {
      const f32x4 t0 = __builtin_bit_cast(f32x4, t[0][n]), t1 = __builtin_bit_cast(f32x4, t[1][n]), t2 = __builtin_bit_cast(f32x4, t[2][n]);
      const f32x4 p0 = sp == 0 ? own[n] : t0, p1 = sp == 1 ? own[n] : (sp < 1 ? t0 : t1), p2 = sp == 2 ? own[n] : (sp < 2 ? t1 : t2), p3 = sp == 3 ? own[n] : t2;
      const f32x4 a = ((p0 + p1) + p2) + p3;
      u32x2 w = {cvtpk(a[0] + bflo(xx[n][0]) + bflo(bb[n][0]), a[1] + bfhi(xx[n][0]) + bfhi(bb[n][0])), cvtpk(a[2] + bflo(xx[n][1]) + bflo(bb[n][1]), a[3] + bfhi(xx[n][1]) + bfhi(bb[n][1]))};
      *(u32x2*)(out + roff + 16 * n) = w;
    }
  }
  STAMP(2, 32); STAMPR(2, 61);
}

extern "C" void kernel_launch(void* const* d_in, const int* in_sizes, int n_in, void* d_out, int out_size, void* d_ws, size_t ws_size,
                              hipStream_t stream) {
  const u16* x = (const u16*)d_in[0];
  const u16* ckv = (const u16*)d_in[1];
  const u16* wq = (const u16*)d_in[2];
  const u16* wuk = (const u16*)d_in[3];
  const u16* wuv = (const u16*)d_in[4];
  const u16* wo = (const u16*)d_in[5];
  const u16* w1 = (const u16*)d_in[7];
  const u16* b1 = (const u16*)d_in[8];
  const u16* w2 = (const u16*)d_in[9];
  const u16* b2 = (const u16*)d_in[10];
  char* ws = (char*)d_ws;
  u16* Kws = (u16*)(ws + OFF_K); u16* Vws = (u16*)(ws + OFF_V); u16* xn_t = (u16*)(ws + OFF_XN); u16* Abuf = (u16*)(ws + OFF_A);
  float* slabs = (float*)(ws + OFF_SLAB);
  (void)hipFuncSetAttribute((const void*)k1_kv, hipFuncAttributeMaxDynamicSharedMemorySize, K1_LDS);
  (void)hipFuncSetAttribute((const void*)k2_attn, hipFuncAttributeMaxDynamicSharedMemorySize, K2_LDS);
  (void)hipFuncSetAttribute((const void*)k3_out, hipFuncAttributeMaxDynamicSharedMemorySize, K3_LDS);
  k1_kv<<<256, 512, K1_LDS, stream>>>(x, ckv, wuk, wuv, Kws, Vws, xn_t, wq, w1);
  k2_attn<<<256, 512, K2_LDS, stream>>>(xn_t, wq, w1, b1, Kws, Vws, Abuf, slabs, wo, w2);
  k3_out<<<256, 512, K3_LDS, stream>>>(Abuf, wo, w2, x, b2, slabs, (u16*)d_out);
}
```

```cpp
#include <hip/hip_runtime.h>
#include <hip/hip_bf16.h>
#include <stdint.h>
#include <type_traits>

typedef unsigned short u16;
typedef short bf16x8 __attribute__((ext_vector_type(8)));
typedef short s16x4 __attribute__((ext_vector_type(4)));
typedef short v4i16_t __attribute__((ext_vector_type(4)));
typedef float f32x2 __attribute__((ext_vector_type(2)));
typedef float f32x4 __attribute__((ext_vector_type(4)));
typedef float f32x16 __attribute__((ext_vector_type(16)));
typedef unsigned u32x4 __attribute__((ext_vector_type(4)));
typedef unsigned u32x2 __attribute__((ext_vector_type(2)));
typedef __bf16 bf16x2_t __attribute__((ext_vector_type(2)));
#define LAS __attribute__((address_space(3)))
#define DI __device__ __forceinline__

constexpr int SEQ = 2048, DM = 512, NH = 32, HD = 128, FF = 1792, KTOT = NH * HD + FF, NKT = KTOT / 64;
constexpr size_t MiB = 1024 * 1024;
constexpr unsigned SENT = 0xffffffffu;
constexpr size_t OFF_K = 0, OFF_V = 16 * MiB, OFF_XN = 32 * MiB, OFF_A = 34 * MiB, OFF_SLAB = 58 * MiB, OFF_CNT = 74 * MiB;

DI unsigned cvtpk(float lo, float hi) { f32x2 v = {lo, hi}; bf16x2_t b = __builtin_convertvector(v, bf16x2_t); return __builtin_bit_cast(unsigned, b); }
DI float rbf(float x) { return __uint_as_float(cvtpk(x, x) & 0xffff0000u); }
DI float bflo(unsigned w) { return __uint_as_float(w << 16); }
DI float bfhi(unsigned w) { return __uint_as_float(w & 0xffff0000u); }
DI s16x4 vtr(LAS const char* p) { return __builtin_bit_cast(s16x4, __builtin_amdgcn_ds_read_tr16_b64_v4i16((LAS v4i16_t*)p)); }
DI bf16x8 vtr2(LAS const char* p0, LAS const char* p1) { s16x4 a = vtr(p0), b = vtr(p1); return __builtin_shufflevector(a, b, 0, 1, 2, 3, 4, 5, 6, 7); }
DI float max3f(float a, float b, float c) { float r; asm("v_max3_f32 %0, %1, %2, %3" : "=v"(r) : "v"(a), "v"(b), "v"(c)); return r; }
DI float max2f(float a, float b) { float r; asm("v_max_f32_e32 %0, %1, %2" : "=v"(r) : "v"(a), "v"(b)); return r; }
DI float fadd_s(float a, float b) { float r; asm("v_add_f32_e32 %0, %1, %2" : "=v"(r) : "v"(a), "v"(b)); return r; }
DI int rfl(int x) { return __builtin_amdgcn_readfirstlane(x); }
#define SCHED __builtin_amdgcn_sched_barrier(0)
#define WAIT_V(n) asm volatile("s_waitcnt vmcnt(" #n ")" ::: "memory")
#define WAIT_L(n) asm volatile("s_waitcnt lgkmcnt(" #n ")" ::: "memory")
#define BAR __builtin_amdgcn_s_barrier()
#define MFMA32(a, b, c) __builtin_amdgcn_mfma_f32_32x32x16_bf16((a), (b), (c), 0, 0, 0)
#define MFMA16(a, b, c) __builtin_amdgcn_mfma_f32_16x16x32_bf16((a), (b), (c), 0, 0, 0)
#define STAMP(kid, i) do {} while (0)
#define STAMPR(kid, i) do {} while (0)

DI void dma1k(const void* sbase, unsigned voff, unsigned ldsdst) {
  unsigned sv;
  asm volatile("s_mov_b32 %0, m0\n\ts_mov_b32 m0, %3\n\ts_nop 0\n\t"
               "global_load_lds_dwordx4 %1, %2\n\t"
               "s_mov_b32 m0, %0" : "=&s"(sv) : "v"(voff), "s"(sbase), "s"(ldsdst) : "memory");
}
DI void touch64(const void* p, unsigned ldsdump) {
  unsigned sv;
  asm volatile("s_mov_b32 %0, m0\n\ts_mov_b32 m0, %2\n\ts_nop 0\n\t"
               "global_load_lds_dword %1, off\n\t"
               "s_mov_b32 m0, %0" : "=&s"(sv) : "v"(p), "s"(ldsdump) : "memory");
}
DI void dma2k(const void* sbase, unsigned voff, unsigned ldsdst) {
  unsigned sv;
  asm volatile("s_mov_b32 %0, m0\n\ts_mov_b32 m0, %3\n\ts_nop 0\n\t"
               "global_load_lds_dwordx4 %1, %2\n\t"
               "global_load_lds_dwordx4 %1, %2 offset:1024\n\t"
               "s_mov_b32 m0, %0" : "=&s"(sv) : "v"(voff), "s"(sbase), "s"(ldsdst) : "memory");
}
DI void dma4k(const void* sbase, unsigned voff, unsigned ldsdst) {
  unsigned sv;
  asm volatile("s_mov_b32 %0, m0\n\ts_mov_b32 m0, %3\n\ts_nop 0\n\t"
               "global_load_lds_dwordx4 %1, %2\n\t"
               "global_load_lds_dwordx4 %1, %2 offset:1024\n\t"
               "global_load_lds_dwordx4 %1, %2 offset:2048\n\t"
               "global_load_lds_dwordx4 %1, %2 offset:3072\n\t"
               "s_mov_b32 m0, %0" : "=&s"(sv) : "v"(voff), "s"(sbase), "s"(ldsdst) : "memory");
}

constexpr int K1_HTB = 128 * 64 * 2, K1_LDS = 8 * K1_HTB;
DI int lds_byte(int r, int c) { const int st = (r >> 4) * 2 + (c >> 5), rr = r & 15, cc = c & 31, ob = rr * 64 + cc * 2; return st * 1024 + (ob ^ (((ob >> 9) & 1) << 5)); }
DI void stage_rc(int b, int& R, int& C) { const int st = b / 1024, sb = b % 1024, swz = sb ^ (((sb >> 9) & 1) << 5); R = (st >> 1) * 16 + swz / 64; C = (st & 1) * 32 + (swz % 64) / 2; }

template <bool ISV>
DI void k1_body(const u16* __restrict__ A, const u16* __restrict__ W, u16* __restrict__ out, int pm, int pn, LAS char* lds,
                const u16* __restrict__ xrow, u16* __restrict__ xn_t, int row) {
  const int tid = threadIdx.x, wid = rfl(tid >> 6), lane = tid & 63, wr = wid >> 2, wc = wid & 3, fr = lane & 15, fq = lane >> 4;
  STAMPR(0, 62); STAMP(0, 0);
  u32x4 xv;
  asm volatile("global_load_dwordx4 %0, %1, off" : "=v"(xv) : "v"(xrow + lane * 8) : "memory");
  unsigned voffA[2], voffB[2];
#pragma unroll
  for (int i = 0; i < 2; ++i) {
    int R, C; stage_rc(tid * 16 + i * 8192, R, C); voffA[i] = (unsigned)(R * DM + C) * 2u;
    const int b = tid * 16 + i * 8192, kb = b >> 11, nh = (b >> 10) & 1, kr = (b >> 7) & 7, c16 = (b >> 4) & 7;
    const int slot = (c16 >> 1) ^ (((kr >> 1) & 1) | ((kb & 1) << 1));
    voffB[i] = (unsigned)((8 * kb + kr) * 4096 + 64 * nh + 16 * slot + 8 * (c16 & 1)) * 2u;
  }
  const unsigned ldsw = (unsigned)wid * 1024u;
  const int aoff = lds_byte(wr * 64 + fr, fq * 8);
  const int boff0 = fq * 2048 + (wc >> 1) * 1024 + ((lane >> 2) & 3) * 128 + (((2 * (wc & 1)) ^ (((lane >> 3) & 1) | ((fq & 1) << 1))) * 32) + 8 * (lane & 3), boff1 = boff0 ^ 32;
  const char* cA = (const char*)A + (size_t)pm * 256 * DM * 2;
  const char* cB = (const char*)W + (size_t)pn * 256 * 2;
  constexpr size_t AK = 64 * 2, AH = (size_t)128 * DM * 2, BK_ = (size_t)64 * 4096 * 2, BH = 128 * 2;
#define SA(b, h) (((b) * 2 + (h)) * K1_HTB)
#define SB(b, h) ((4 + (b) * 2 + (h)) * K1_HTB)
#define STAGE(bufoff, gbase, voff) do { _Pragma("unroll") for (int _i = 0; _i < 2; ++_i) \
    __builtin_amdgcn_global_load_lds((const unsigned*)((const char*)(gbase) + (voff)[_i]), (LAS unsigned*)(lds + (bufoff) + ldsw + _i * 8192), 16, 0, 0); } while (0)
#define STA(b, h, kt) STAGE(SA(b, h), cA + (h) * AH + (kt) * AK, voffA)
#define STB(b, h, kt) STAGE(SB(b, h), cB + (h) * BH + (kt) * BK_, voffB)
#define LDA(dst, b, h) do { _Pragma("unroll") for (int m = 0; m < 4; ++m) _Pragma("unroll") for (int k = 0; k < 2; ++k) dst[m][k] = *(const LAS bf16x8*)(lds + SA(b, h) + aoff + m * 2048 + k * 1024); } while (0)
#define LDB(dst, b, h) do { _Pragma("unroll") for (int n = 0; n < 2; ++n) _Pragma("unroll") for (int k = 0; k < 2; ++k) \
    dst[n][k] = vtr2(lds + SB(b, h) + (n ? boff1 : boff0) + k * 8192, lds + SB(b, h) + (n ? boff1 : boff0) + k * 8192 + 512); } while (0)
#define MMA(ai, bj, At, Bt) do { __builtin_amdgcn_s_setprio(1); _Pragma("unroll") for (int m = 0; m < 4; ++m) _Pragma("unroll") for (int n = 0; n < 2; ++n) _Pragma("unroll") for (int k = 0; k < 2; ++k) \
    acc[ai][bj][m][n] = ISV ? MFMA16(At[m][k], Bt[n][k], acc[ai][bj][m][n]) : MFMA16(Bt[n][k], At[m][k], acc[ai][bj][m][n]); __builtin_amdgcn_s_setprio(0); } while (0)
  f32x4 acc[2][2][4][2];
#pragma unroll
  for (int a = 0; a < 2; ++a)
#pragma unroll
    for (int b = 0; b < 2; ++b)
#pragma unroll
      for (int m = 0; m < 4; ++m)
#pragma unroll
        for (int n = 0; n < 2; ++n) acc[a][b][m][n] = (f32x4){0.f, 0.f, 0.f, 0.f};
  bf16x8 At[4][2], B0[2][2], B1[2][2];
  constexpr int nt = DM / 64;
  STB(0, 0, 0); STA(0, 0, 0); STB(0, 1, 0); STA(0, 1, 0);
  if (wr == 1) BAR;
  asm volatile("s_waitcnt vmcnt(4)" : "+v"(xv) :: "memory");
  BAR;
  {
    float v[8];
#pragma unroll
    for (int i = 0; i < 4; ++i) { v[2 * i] = bflo(xv[i]); v[2 * i + 1] = bfhi(xv[i]); }
    float s = 0.f;
#pragma unroll
    for (int i = 0; i < 8; ++i) s += v[i];
#pragma unroll
    for (int o = 32; o >= 1; o >>= 1) s += __shfl_xor(s, o);
    const float mean = s * (1.0f / DM);
    float q = 0.f;
#pragma unroll
    for (int i = 0; i < 8; ++i) { const float d = v[i] - mean; q += d * d; }
#pragma unroll
    for (int o = 32; o >= 1; o >>= 1) q += __shfl_xor(q, o);
    const float mb = rbf(mean), vb = rbf(q * (1.0f / DM));
    const float den = rbf(sqrtf(rbf(vb + 1e-5f)));
    u32x4 o4;
#pragma unroll
    for (int i = 0; i < 4; ++i) o4[i] = cvtpk(rbf(v[2 * i] - mb) / den, rbf(v[2 * i + 1] - mb) / den);
    *(u32x4*)(xn_t + ((size_t)((row >> 4) * 16 + (lane >> 2)) * 64 + (lane & 3) * 16 + (row & 15)) * 8) = o4;
  }
  STB(1, 0, 1); STA(1, 0, 1); STB(1, 1, 1);
  WAIT_V(6); BAR;
  STAMP(0, 1);
  for (int t = 0; t < nt - 2; t += 2) {
    LDB(B0, 0, 0); SCHED; LDA(At, 0, 0); STA(1, 1, t + 1);
    WAIT_L(8); BAR; WAIT_L(0); MMA(0, 0, At, B0); BAR; SCHED;
    LDB(B1, 0, 1); STB(0, 0, t + 2);
    BAR; WAIT_L(0); MMA(0, 1, At, B1); BAR;
    LDA(At, 0, 1); STA(0, 0, t + 2);
    BAR; WAIT_L(0); MMA(1, 0, At, B0); BAR; SCHED;
    STB(0, 1, t + 2);
    WAIT_V(6); BAR; MMA(1, 1, At, B1); BAR;
    LDB(B0, 1, 0); SCHED; LDA(At, 1, 0); STA(0, 1, t + 2);
    WAIT_L(8); BAR; WAIT_L(0); MMA(0, 0, At, B0); BAR; SCHED;
    LDB(B1, 1, 1); STB(1, 0, t + 3);
    BAR; WAIT_L(0); MMA(0, 1, At, B1); BAR;
    LDA(At, 1, 1); STA(1, 0, t + 3);
    BAR; WAIT_L(0); MMA(1, 0, At, B0); BAR; SCHED;
    STB(1, 1, t + 3);
    WAIT_V(6); BAR; MMA(1, 1, At, B1); BAR;
  }
  { LDB(B0, 0, 0); WAIT_V(0); LDA(At, 0, 0); STA(1, 1, nt - 1);
    BAR; WAIT_L(0); MMA(0, 0, At, B0); BAR;
    LDB(B1, 0, 1); BAR; WAIT_L(0); MMA(0, 1, At, B1); BAR;
    LDA(At, 0, 1); WAIT_V(4); BAR; WAIT_L(0); MMA(1, 0, At, B0); MMA(1, 1, At, B1); BAR; }
  { LDB(B0, 1, 0); LDA(At, 1, 0); WAIT_V(2); BAR; WAIT_L(0); MMA(0, 0, At, B0); BAR;
    LDB(B1, 1, 1); WAIT_V(0); BAR; WAIT_L(0); MMA(0, 1, At, B1); BAR;
    LDA(At, 1, 1); BAR; WAIT_L(0); MMA(1, 0, At, B0); MMA(1, 1, At, B1); BAR; }
  if (wr == 0) BAR;
  STAMP(0, 2);
#pragma unroll
  for (int ai = 0; ai < 2; ++ai)
#pragma unroll
    for (int bj = 0; bj < 2; ++bj) {
      const int head = 2 * pn + bj, tile = 4 * pm + 2 * ai + wr;
      const __amdgpu_buffer_rsrc_t trs = __builtin_amdgcn_make_buffer_rsrc((void*)(out + (size_t)(head * 32 + tile) * 8192), (short)0, 16384, 0x00020000);
      if (!ISV) {
#pragma unroll
        for (int m = 0; m < 4; ++m) {
          const f32x4 a0 = acc[ai][bj][m][0], a1 = acc[ai][bj][m][1];
          u32x4 w = {cvtpk(a0[0], a0[1]), cvtpk(a0[2], a0[3]), cvtpk(a1[0], a1[1]), cvtpk(a1[2], a1[3])};
          __builtin_amdgcn_raw_buffer_store_b128(w, trs, ((4 * wc + fq) * 64 + 16 * m + fr) * 16, 0, 0);
        }
      } else {
#pragma unroll
        for (int mp = 0; mp < 2; ++mp)
#pragma unroll
          for (int n = 0; n < 2; ++n) {
            const f32x4 a0 = acc[ai][bj][2 * mp][n], a1 = acc[ai][bj][2 * mp + 1][n];
            u32x4 w = {cvtpk(a0[0], a0[1]), cvtpk(a0[2], a0[3]), cvtpk(a1[0], a1[1]), cvtpk(a1[2], a1[3])};
            __builtin_amdgcn_raw_buffer_store_b128(w, trs, ((4 * mp + fq) * 128 + 32 * wc + 16 * n + fr) * 16, 0, 0);
          }
      }
    }
  STAMP(0, 3); STAMPR(0, 63);
#undef SA
#undef SB
#undef STAGE
#undef STA
#undef STB
#undef LDA
#undef LDB
#undef MMA
}

__global__ __launch_bounds__(512, 2) void k1_kv(const u16* __restrict__ x, const u16* __restrict__ ckv, const u16* __restrict__ wuk, const u16* __restrict__ wuv,
                                                u16* __restrict__ Kws, u16* __restrict__ Vws, u16* __restrict__ xn_t) {
  extern __shared__ __attribute__((aligned(16))) char smem[];
  const int b = blockIdx.x, xcd = b & 7, idx = b >> 3;
  const int pnn = ((idx >> 4) << 4) + xcd * 2 + ((idx >> 3) & 1), pm = idx & 7;
  const int row = b * 8 + rfl(threadIdx.x >> 6);
  if (pnn < 16) k1_body<false>(ckv, wuk, Kws, pm, pnn, (LAS char*)smem, x + (size_t)row * DM, xn_t, row);
  else          k1_body<true>(ckv, wuv, Vws, pm, pnn - 16, (LAS char*)smem, x + (size_t)row * DM, xn_t, row);
}

#define LSTAMP(k) do {} while (0)
constexpr int SLOT = 16384, K2_LDS = 10 * SLOT;
constexpr float QSCALE = 0.08838834764831845f * 1.4426950408889634f;
constexpr float THR = 32.f;
#define WBAR(N) do { asm volatile("s_waitcnt vmcnt(" #N ") lgkmcnt(0)\n\ts_barrier" ::: "memory"); SCHED; } while (0)
#define LBAR() do { asm volatile("s_waitcnt lgkmcnt(0)\n\ts_barrier" ::: "memory"); SCHED; } while (0)
#define VBAR(N) do { asm volatile("s_waitcnt vmcnt(" #N ")\n\ts_barrier" ::: "memory"); SCHED; } while (0)
DI float xg_max(float v) { v = fmaxf(v, __shfl_xor(v, 16)); return fmaxf(v, __shfl_xor(v, 32)); }
DI float xg_sum(float v) { v += __shfl_xor(v, 16); return v + __shfl_xor(v, 32); }
DI bf16x8 pack8(const f32x4& a, const f32x4& b, float sc) {
  return __builtin_bit_cast(bf16x8, (u32x4){cvtpk(a[0] * sc, a[1] * sc), cvtpk(a[2] * sc, a[3] * sc), cvtpk(b[0] * sc, b[1] * sc), cvtpk(b[2] * sc, b[3] * sc)});
}

__global__ __launch_bounds__(512, 2) void k2_attn(const u16* __restrict__ xn_t, const u16* __restrict__ Wq, const u16* __restrict__ W1, const u16* __restrict__ b1,
                                                  const u16* __restrict__ Kws, const u16* __restrict__ Vws, u16* __restrict__ Abuf, unsigned* __restrict__ slabs,
                                                  const u16* __restrict__ Wo, const u16* __restrict__ W2) {
  extern __shared__ __attribute__((aligned(16))) char smem[];
  LAS char* lds = (LAS char*)smem;
  const unsigned lds0 = (unsigned)(uintptr_t)lds;
  const int tid = threadIdx.x, lane = tid & 63, wid = rfl(tid >> 6), grp = wid >> 2, wq = wid & 3, c = lane & 15, g = lane >> 4;
  const int b = blockIdx.x, head = (b & 7) * 4 + (b >> 6), x = (b >> 3) & 7;
  const bool doF = head < 28;
  const int fh = doF ? head : 27;
  const int sw = 2 * x + 2;
  const int blk0 = grp ? 15 - x : x;
  const char* Kh = (const char*)Kws + (size_t)head * 32 * SLOT;
  const char* Vh = (const char*)Vws + (size_t)head * 32 * SLOT;
  const unsigned vlin = (unsigned)(wq * 4096 + lane * 16);
  const int tb0 = grp ? 15 - 2 * x : 0;
  const __amdgpu_buffer_rsrc_t arsrc = __builtin_amdgcn_make_buffer_rsrc((void*)Abuf, (short)0, 16 * NKT * 16384, 0x00020000);
  const int cvoff = (32 * wq + c) * 16 + g * 2048;

  bf16x8 qf[4][2];
  STAMPR(1, 62); STAMP(1, 0);
  auto prologue = [&](auto ftag) __attribute__((always_inline)) {
    constexpr bool DOF = decltype(ftag)::value;
    const char* xbase = (const char*)xn_t + (size_t)(blk0 * 8 + 2 * wq) * 16384;
    const unsigned xvo = lane * 16;
    const int dsl = 16 * (((lane >> 1) & 3) ^ (((lane >> 4) & 1) | ((wid & 1) << 1))) + 8 * (lane & 1);
    const unsigned voffq = (unsigned)((8 * wid + (lane >> 3)) * 4096 + head * 128 + dsl) * 2u;
    const unsigned voff1 = (unsigned)((8 * wid + (lane >> 3)) * FF + fh * 64 + dsl) * 2u;
    constexpr int CH = 24576;
    auto stage_w = [&](int ch) __attribute__((always_inline)) {
      const char* q0 = (const char*)Wq + (size_t)ch * 64 * 4096 * 2;
      const char* f0 = (const char*)W1 + (size_t)ch * 64 * FF * 2;
      const unsigned d0 = lds0 + (ch & 3) * CH + wid * 3072;
      dma1k(q0, voffq, d0); dma1k(q0 + 128, voffq, d0 + 1024); dma1k(f0, voff1, d0 + 2048);
    };
    int wb[4];
#pragma unroll
    for (int sl = 0; sl < 4; ++sl) wb[sl] = g * 3072 + ((lane >> 2) & 3) * 128 + ((sl ^ (((lane >> 3) & 1) | ((g & 1) << 1))) * 32) + 8 * (lane & 3);
#define WFRAG(ch, ks, t) vtr2(lds + ((ch) & 3) * CH + wb[(t) & 3] + (ks) * 12288 + ((t) >> 2) * 1024, lds + ((ch) & 3) * CH + wb[(t) & 3] + (ks) * 12288 + ((t) >> 2) * 1024 + 512)
    f32x4 aq[12][2];
#pragma unroll
    for (int t = 0; t < 12; ++t) { aq[t][0] = (f32x4){0.f, 0.f, 0.f, 0.f}; aq[t][1] = (f32x4){0.f, 0.f, 0.f, 0.f}; }
    bf16x8 xr[4][4];
#define XLOAD(ch) asm volatile("global_load_dwordx4 %0, %4, %5\n\tglobal_load_dwordx4 %1, %4, %5 offset:1024\n\tglobal_load_dwordx4 %2, %4, %6\n\tglobal_load_dwordx4 %3, %4, %6 offset:1024" \
      : "=&v"(xr[(ch) & 3][0]), "=&v"(xr[(ch) & 3][1]), "=&v"(xr[(ch) & 3][2]), "=&v"(xr[(ch) & 3][3]) : "v"(xvo), "s"(xbase + (ch) * 2048), "s"(xbase + 16384 + (ch) * 2048) : "memory")
#define CWAIT(ch, N) do { asm volatile("s_waitcnt vmcnt(" #N ") lgkmcnt(0)\n\ts_barrier" : "+v"(xr[(ch) & 3][0]), "+v"(xr[(ch) & 3][1]), "+v"(xr[(ch) & 3][2]), "+v"(xr[(ch) & 3][3]) :: "memory"); SCHED; } while (0)
    constexpr int WPD = 6;
    bf16x8 wa[24 + WPD];
    auto compute = [&](int ch, auto&& act) __attribute__((always_inline)) {
#pragma unroll
      for (int i = 0; i < 24; ++i) {
        const int ks = i / 12, t = i % 12, in = i + WPD, chn = in < 24 ? ch : ch + 1, ksn = (in % 24) / 12, tn = in % 12;
        if ((in < 24 || ch < 7) && (tn < 8 || DOF)) wa[in] = WFRAG(chn, ksn, tn);
        if (t < 8 || DOF) { aq[t][0] = MFMA16(wa[i], xr[ch & 3][ks], aq[t][0]); aq[t][1] = MFMA16(wa[i], xr[ch & 3][2 + ks], aq[t][1]); }
        if (i % 6 == 1) act(i / 6);
        SCHED;
      }
#pragma unroll
      for (int j = 0; j < WPD; ++j) wa[j] = wa[24 + j];
    };
    u32x2 bb[4];
    const u16* b1p = b1 + fh * 64; const int bvo = g * 8;
    XLOAD(0); stage_w(0); XLOAD(1); stage_w(1); XLOAD(2); stage_w(2);
    CWAIT(0, 14); STAMP(1, 1);
#pragma unroll
    for (int j = 0; j < WPD; ++j) wa[j] = WFRAG(0, 0, j);
    CWAIT(1, 7); compute(0, [&](int i) __attribute__((always_inline)) { if (i == 0) XLOAD(3); if (i == 1) stage_w(3); });
    CWAIT(2, 7); compute(1, [&](int i) __attribute__((always_inline)) { if (i == 0) XLOAD(4); if (i == 1) stage_w(4); });
    CWAIT(3, 7); compute(2, [&](int i) __attribute__((always_inline)) { if (i == 0) XLOAD(5); if (i == 1) stage_w(5); });
    CWAIT(4, 7); compute(3, [&](int i) __attribute__((always_inline)) { if (i == 0) XLOAD(6); if (i == 1) stage_w(6); });
    CWAIT(5, 7); compute(4, [&](int i) __attribute__((always_inline)) { if (i == 0) XLOAD(7); if (i == 1) stage_w(7); });
    CWAIT(6, 7);
    compute(5, [&](int i) __attribute__((always_inline)) {
      if (i == 0) asm volatile("global_load_dwordx2 %0, %4, %5\n\tglobal_load_dwordx2 %1, %4, %5 offset:32\n\tglobal_load_dwordx2 %2, %4, %5 offset:64\n\tglobal_load_dwordx2 %3, %4, %5 offset:96"
                               : "=&v"(bb[0]), "=&v"(bb[1]), "=&v"(bb[2]), "=&v"(bb[3]) : "v"(bvo), "s"(b1p) : "memory");
    });
    CWAIT(7, 4);
    compute(6, [&](int i) __attribute__((always_inline)) {
      if (i == 0) dma4k(Kh + (size_t)tb0 * SLOT, vlin, lds0 + (grp ? 6 : 9) * SLOT + wq * 4096);
      if (i == 1) dma4k(Vh + (size_t)tb0 * SLOT, vlin, lds0 + (grp ? 7 : 0) * SLOT + wq * 4096);
      if (i == 2) dma4k(Kh + (size_t)(tb0 + 1) * SLOT, vlin, lds0 + (grp ? 8 : 1) * SLOT + wq * 4096);
    });
    compute(7, [&](int) __attribute__((always_inline)) {});
    STAMP(1, 2);
    asm volatile("s_waitcnt vmcnt(8) lgkmcnt(0)\n\ts_barrier" : "+v"(bb[0]), "+v"(bb[1]), "+v"(bb[2]), "+v"(bb[3]) :: "memory"); SCHED;
#undef XLOAD
#undef CWAIT
#undef WFRAG
#pragma unroll
    for (int u = 0; u < 4; ++u) { qf[u][0] = pack8(aq[2 * u][0], aq[2 * u + 1][0], QSCALE); qf[u][1] = pack8(aq[2 * u][1], aq[2 * u + 1][1], QSCALE); }
    if (grp == 1) {
      LAS char* st = lds + 4 * SLOT + wq * 8192 + lane * 16;
#pragma unroll
      for (int i = 0; i < 8; ++i) *(LAS bf16x8*)(st + i * 1024) = qf[i >> 1][i & 1];
    }
    if constexpr (DOF) {
      const int sbase = (blk0 * NKT + 64 + head) * 16384;
#pragma unroll
      for (int v = 0; v < 2; ++v)
#pragma unroll
        for (int qt = 0; qt < 2; ++qt) {
          u32x4 w;
#pragma unroll
          for (int hf = 0; hf < 2; ++hf) {
            const f32x4 a = aq[8 + 2 * v + hf][qt]; const u32x2 bv = bb[2 * v + hf];
            float t0 = rbf(rbf(a[0]) + bflo(bv[0])), t1 = rbf(rbf(a[1]) + bfhi(bv[0])), t2 = rbf(rbf(a[2]) + bflo(bv[1])), t3 = rbf(rbf(a[3]) + bfhi(bv[1]));
            t0 = t0 > 0.f ? t0 : 0.f; t1 = t1 > 0.f ? t1 : 0.f; t2 = t2 > 0.f ? t2 : 0.f; t3 = t3 > 0.f ? t3 : 0.f;
            w[2 * hf] = cvtpk(t0 * t0, t1 * t1); w[2 * hf + 1] = cvtpk(t2 * t2, t3 * t3);
          }
          __builtin_amdgcn_raw_buffer_store_b128(w, arsrc, cvoff + sbase + v * 8192 + qt * 256, 0, 0);
        }
    }
  };
  if (doF) prologue(std::true_type{}); else prologue(std::false_type{});
  {
    const __amdgpu_buffer_rsrc_t frs = __builtin_amdgcn_make_buffer_rsrc((void*)(slabs + (size_t)b * 6144), (short)0, 24576, 0x00020000);
#pragma unroll
    for (int i = 0; i < 3; ++i) __builtin_amdgcn_raw_buffer_store_b128((u32x4){SENT, SENT, SENT, SENT}, frs, tid * 16 + i * 8192, 0, 17);
  }
  const int klane = g * 1024 + c * 16, vlane = g * 2048 + c * 16;
  const int kslot0 = (grp ? 6 : 9) * SLOT, kslot1 = (grp ? 8 : 1) * SLOT, vslot0 = (grp ? 7 : 0) * SLOT, vslot1 = (grp ? 3 : 2) * SLOT;
  f32x4 o[8][2], sc[4][2], negm[2];
#pragma unroll
  for (int i = 0; i < 8; ++i) { o[i][0] = (f32x4){0.f, 0.f, 0.f, 0.f}; o[i][1] = (f32x4){0.f, 0.f, 0.f, 0.f}; }
  negm[0] = (f32x4){0.f, 0.f, 0.f, 0.f}; negm[1] = (f32x4){0.f, 0.f, 0.f, 0.f};
  bf16x8 pf[2][2];
  float mref[2] = {0.f, 0.f};
  f32x4 lacc[2] = {(f32x4){0.f, 0.f, 0.f, 0.f}, (f32x4){0.f, 0.f, 0.f, 0.f}};
  bf16x8 ones = {0x3f80, 0x3f80, 0x3f80, 0x3f80, 0x3f80, 0x3f80, 0x3f80, 0x3f80};
  asm volatile("" : "+v"(ones));
  auto store_ctx = [&](int blk, float inv0, float inv1) __attribute__((always_inline)) {
    const int sbase = (blk * NKT + 2 * head) * 16384;
#pragma unroll
    for (int v = 0; v < 4; ++v)
#pragma unroll
      for (int qt = 0; qt < 2; ++qt)
        __builtin_amdgcn_raw_buffer_store_b128(__builtin_bit_cast(u32x4, pack8(o[2 * v][qt], o[2 * v + 1][qt], qt ? inv1 : inv0)), arsrc, cvoff + sbase + v * 8192 + qt * 256, 0, 0);
  };
  constexpr int PD = 6;
  bf16x8 fr_[32];
  const int swk = grp ? 99 : sw;
  int ktile = tb0 + 1, vtile = tb0;
  unsigned kvo = 0, vvo = 0;
#define FRD(i) (((i) < 16) ? *(LAS const bf16x8*)(kp + ((i) >> 2) * 4096 + ((i) & 3) * 256) : *(LAS const bf16x8*)(vp + (((i) - 16) >> 3) * 8192 + (((i) - 16) & 7) * 256))
#define SETM0(v) asm volatile("s_mov_b32 m0, %0\n\ts_nop 0" :: "s"(v) : "memory")
#define DMAI(vo, base, off) asm volatile("global_load_lds_dwordx4 %0, %1 offset:" #off :: "v"(vo), "s"(base) : "memory")
#define MBODY(i) do { if (i < 16) { sc[i & 3][0] = MFMA16(fr_[i], qf[i >> 2][0], i < 4 ? negm[0] : sc[i & 3][0]); sc[i & 3][1] = MFMA16(fr_[i], qf[i >> 2][1], i < 4 ? negm[1] : sc[i & 3][1]); } \
      else { o[(i - 16) & 7][0] = MFMA16(fr_[i], pf[(i - 16) >> 3][0], o[(i - 16) & 7][0]); o[(i - 16) & 7][1] = MFMA16(fr_[i], pf[(i - 16) >> 3][1], o[(i - 16) & 7][1]); \
        if (((i - 16) & 7) == 3) lacc[0] = MFMA16(ones, pf[(i - 16) >> 3][0], lacc[0]); if (((i - 16) & 7) == 5) lacc[1] = MFMA16(ones, pf[(i - 16) >> 3][1], lacc[1]); } } while (0)
#define KPIECE(s_, j) do { if ((j) == 0) { ktile = ((s_) + 2 == swk) ? 0 : ktile + 1; kvo = vlin + (unsigned)min(ktile, 31) * SLOT; SETM0(lds0 + (((s_) & 1) ? kslot1 : kslot0) + wq * 4096); DMAI(kvo, Kh, 0); } \
    if ((j) == 1) DMAI(kvo, Kh, 1024); if ((j) == 2) DMAI(kvo, Kh, 2048); if ((j) == 3) DMAI(kvo, Kh, 3072); } while (0)
#define VPIECE(s_, j) do { if ((j) == 0) { SETM0(lds0 + (((s_) & 1) ? vslot1 : vslot0) + wq * 4096); DMAI(vvo, Vh, 0); } \
    if ((j) == 1) DMAI(vvo, Vh, 1024); if ((j) == 2) DMAI(vvo, Vh, 2048); if ((j) == 3) DMAI(vvo, Vh, 3072); } while (0)
#define MPRE(s_, I0) do { \
    LAS const char* kp = lds + (((s_) & 1) ? kslot1 : kslot0) + klane; \
    LAS const char* vp = lds + (((s_) & 1) ? vslot0 : vslot1) + vlane; \
    _Pragma("unroll") for (int i = I0; i < I0 + PD; ++i) fr_[i] = FRD(i); \
    (void)kp; (void)vp; } while (0)
#define MSLOT(s_, I0, I1, DM) do { \
    LAS const char* kp = lds + (((s_) & 1) ? kslot1 : kslot0) + klane; \
    LAS const char* vp = lds + (((s_) & 1) ? vslot0 : vslot1) + vlane; \
    if (DM) { vtile = ((s_) == swk) ? 0 : vtile + 1; vvo = vlin + (unsigned)min(vtile, 31) * SLOT; } \
    _Pragma("unroll") for (int i = I0; i < I1; ++i) { \
      if (i + PD < I1) fr_[i + PD] = FRD(i + PD); \
      MBODY(i); \
      if ((DM) && (i & 3) == 2 && i < 16) VPIECE(s_, i >> 2); \
      SCHED; \
    } (void)kp; (void)vp; } while (0)
#define SCV(i) sc[((i) >> 2) & 3][(i) >> 4][(i) & 3]
  auto sslot = [&](const int s, const bool SLOW) __attribute__((always_inline)) {
    KPIECE(s, 0);
    bool first = false;
    if (__builtin_expect(SLOW, 0)) {
      const bool swst = (s == swk);
      if (swst) {
        store_ctx(x, 1.0f / lacc[0][0], 1.0f / lacc[1][0]);
#pragma unroll
        for (int i = 0; i < 8; ++i) { o[i][0] = (f32x4){0.f, 0.f, 0.f, 0.f}; o[i][1] = (f32x4){0.f, 0.f, 0.f, 0.f}; }
        lacc[0] = (f32x4){0.f, 0.f, 0.f, 0.f}; lacc[1] = (f32x4){0.f, 0.f, 0.f, 0.f};
      }
      first = (s == 0) || swst;
      const int jb = grp ? s - 15 : s - (sw - 2);
      if (jb >= 0 && jb <= 1) {
        const int dq = 32 * wq + c - 64 * jb - 4 * g;
#pragma unroll
        for (int i = 0; i < 32; ++i) if (16 * ((i >> 2) & 3) + (i & 3) > dq + 16 * (i >> 4)) SCV(i) = -__builtin_inff();
      }
    }
    float pm0 = max3f(SCV(0), SCV(1), SCV(2)), pm1 = max3f(SCV(16), SCV(17), SCV(18));
#pragma unroll
    for (int i = 3; i < 15; i += 2) { pm0 = max3f(pm0, SCV(i), SCV(i + 1)); pm1 = max3f(pm1, SCV(16 + i), SCV(17 + i)); }
    pm0 = max2f(pm0, SCV(15)); pm1 = max2f(pm1, SCV(31));
    KPIECE(s, 1);
    if (__builtin_expect(first || __any(max2f(pm0, pm1) > THR), 0)) {
      const float r0 = xg_max(pm0), r1 = xg_max(pm1);
      const float dl[2] = {first ? r0 : fmaxf(r0, 0.f), first ? r1 : fmaxf(r1, 0.f)};
#pragma unroll
      for (int qt = 0; qt < 2; ++qt) {
        mref[qt] += dl[qt];
#pragma unroll
        for (int kt = 0; kt < 4; ++kt) sc[kt][qt] -= dl[qt];
        if (!first) {
          const float al = __builtin_amdgcn_exp2f(-dl[qt]);
          lacc[qt] *= al;
#pragma unroll
          for (int d = 0; d < 8; ++d) o[d][qt] *= al;
        }
        negm[qt] = (f32x4){-mref[qt], -mref[qt], -mref[qt], -mref[qt]};
      }
    }
    {
      unsigned pw[16];
      SCV(0) = __builtin_amdgcn_exp2f(SCV(0)); SCV(1) = __builtin_amdgcn_exp2f(SCV(1)); SCHED;
#pragma unroll
      for (int i = 0; i < 32; i += 2) {
        if (i + 2 < 32) SCV(i + 2) = __builtin_amdgcn_exp2f(SCV(i + 2));
        pw[i >> 1] = cvtpk(SCV(i), SCV(i + 1));
        if (i + 3 < 32) SCV(i + 3) = __builtin_amdgcn_exp2f(SCV(i + 3));
        if (i == 8) KPIECE(s, 2);
        if (i == 20) KPIECE(s, 3);
        SCHED;
      }
#pragma unroll
      for (int k4 = 0; k4 < 4; ++k4) pf[k4 & 1][k4 >> 1] = __builtin_bit_cast(bf16x8, (u32x4){pw[4 * k4], pw[4 * k4 + 1], pw[4 * k4 + 2], pw[4 * k4 + 3]});
    }
    if (__builtin_expect(SLOW, 0)) {
      if (s + 1 == swk) {
        LAS const char* st = lds + 4 * SLOT + wq * 8192 + lane * 16;
#pragma unroll
        for (int i = 0; i < 8; ++i) qf[i >> 1][i & 1] = *(LAS const bf16x8*)(st + i * 1024);
        mref[0] = 0.f; mref[1] = 0.f; negm[0] = (f32x4){0.f, 0.f, 0.f, 0.f}; negm[1] = (f32x4){0.f, 0.f, 0.f, 0.f};
      }
    }
    asm volatile("" : "+v"(pf[0][0]), "+v"(pf[0][1]), "+v"(pf[1][0]), "+v"(pf[1][1]), "+v"(negm[0]), "+v"(negm[1]));
  };
  auto warm_k3 = [&]() __attribute__((always_inline)) {
#pragma unroll
    for (int i = 0; i < 3; ++i) {
      int sec = (wq * 3 + i) * 64 + lane; sec = sec < 736 ? sec : 735;
      const int kr = 1472 * ((b & 7) >> 1) + 46 * (b >> 3) + (sec >> 4);
      const char* src = (kr < 4096 ? (const char*)Wo + (size_t)kr * 1024 : (const char*)W2 + (size_t)(kr - 4096) * 1024) + (sec & 15) * 64;
      touch64(src, lds0 + 8 * SLOT + wq * 256);
    }
  };
  const unsigned spec = grp ? 0x18001u : (0x0001u | (7u << (sw - 2)));
  LAS char* pb = lds + (wq == 0 ? 9 : (wq == 1 ? 1 : 2 + wq)) * SLOT + lane * 16;
  LAS char* mlp = lds + 2 * SLOT + wq * 1024 + lane * 16;
  STAMP(1, 3);
  if (grp == 1) LBAR();
  MPRE(0, 0); SCHED;
  MSLOT(0, 0, 16, false);
  WBAR(0);
  for (int s = 0; s < 16; ++s) {
    LSTAMP(5);
    sslot(s, (spec >> s) & 1u);
    MPRE(s + 1, 0);
    LSTAMP(6);
    if (__builtin_expect(s == swk, 0)) VBAR(12); else VBAR(4);
    LSTAMP(7);
    MSLOT(s + 1, 0, 32, true);
    LSTAMP(8);
    WBAR(4);
  }
  STAMP(1, 37);
  sslot(16, true);
  WBAR(0);
  if (grp == 1) warm_k3();
  MPRE(17, 16); SCHED;
  MSLOT(17, 16, 32, false);
  STAMP(1, 40);
  if (grp == 0) {
#pragma unroll
    for (int i = 0; i < 16; ++i) *(LAS f32x4*)(pb + i * 1024) = o[i >> 1][i & 1];
    *(LAS f32x4*)mlp = (f32x4){mref[0], lacc[0][0], mref[1], lacc[1][0]};
    LBAR();
  } else {
    const f32x4 ml = *(LAS const f32x4*)mlp;
    float ga[2], gb[2];
#pragma unroll
    for (int qt = 0; qt < 2; ++qt) {
      const float mn = fmaxf(ml[2 * qt], mref[qt]), fa = __builtin_amdgcn_exp2f(ml[2 * qt] - mn), fb = __builtin_amdgcn_exp2f(mref[qt] - mn);
      const float inv = 1.0f / (lacc[qt][0] * fb + ml[2 * qt + 1] * fa);
      ga[qt] = fa * inv; gb[qt] = fb * inv;
    }
#pragma unroll
    for (int i = 0; i < 16; ++i) { const f32x4 pa = *(LAS const f32x4*)(pb + i * 1024); o[i >> 1][i & 1] = o[i >> 1][i & 1] * gb[i & 1] + pa * ga[i & 1]; }
    store_ctx(15 - x, 1.0f, 1.0f);
  }
#undef FRD
#undef MSLOT
#undef MPRE
#undef MBODY
#undef KPIECE
#undef VPIECE
#undef SCV
  STAMP(1, 41); STAMPR(1, 63);
}

constexpr int K3_NS = 5, K3_LDS = K3_NS * 32768;
__global__ __launch_bounds__(512, 2) void k3_out(const u16* __restrict__ Abuf, const u16* __restrict__ Wo, const u16* __restrict__ W2, const u16* __restrict__ x,
                                                 const u16* __restrict__ b2, unsigned* __restrict__ slabs, u16* __restrict__ out) {
  extern __shared__ __attribute__((aligned(16))) char smem[];
  LAS char* lds = (LAS char*)smem;
  const unsigned lds0 = (unsigned)(uintptr_t)lds;
  const int tid = threadIdx.x, lane = tid & 63, wid = rfl(tid >> 6), kh = wid >> 2, wr = (wid >> 1) & 1, wc = wid & 1, fr = lane & 15, fq = lane >> 4;
  const int b = blockIdx.x, xcd = b & 7, idx = b >> 3, sp = xcd >> 1, rt = (xcd & 1) * 8 + (idx >> 2), ct = idx & 3;
  const int kt0 = sp * 23;
  const char* Ab = (const char*)Abuf + (size_t)(rt * NKT + kt0) * 16384;
  const unsigned voffa = (unsigned)(wid * 2048 + lane * 16);
  const unsigned voffb = (unsigned)((32 * (wid >> 2) + 16 * (lane >> 5) + 4 * (wid & 3) + ((lane >> 3) & 3)) * DM + 128 * ct + 16 * (((lane >> 1) & 3) ^ (((lane >> 4) & 1) | ((wid & 1) << 1))) + 8 * (lane & 1)) * 2u;
  auto stage = [&](int it) __attribute__((always_inline)) {
    const int kt = kt0 + it;
    const char* wb = kt < 64 ? (const char*)Wo + (size_t)kt * 64 * DM * 2 : (const char*)W2 + (size_t)(kt - 64) * 64 * DM * 2;
    const unsigned d = lds0 + (it % K3_NS) * 32768 + wid * 2048;
    dma2k(Ab + (size_t)it * 16384, voffa, d);
    dma1k(wb, voffb, d + 16384); dma1k(wb + 128, voffb, d + 16384 + 1024);
  };
  const int aoff = (4 * kh + fq) * 2048 + (wr * 64 + fr) * 16;
  int boff[4];
#pragma unroll
  for (int n = 0; n < 4; ++n) boff[n] = 16384 + (4 * kh + fq) * 2048 + wc * 1024 + ((lane >> 2) & 3) * 128 + ((n ^ (((lane >> 3) & 1) | ((fq & 1) << 1))) * 32) + 8 * (lane & 3);
  f32x4 acc[4][4];
#pragma unroll
  for (int m = 0; m < 4; ++m)
#pragma unroll
    for (int n = 0; n < 4; ++n) acc[m][n] = (f32x4){0.f, 0.f, 0.f, 0.f};
  bf16x8 a0[4], b0[4], a1[4], b1[4];
#define K3_RDB(bf, it_, n) bf[n] = vtr2(lds + ((it_) % K3_NS) * 32768 + boff[n], lds + ((it_) % K3_NS) * 32768 + boff[n] + 512)
#define K3_RDA(af, it_, m) af[m] = *(LAS const bf16x8*)(lds + ((it_) % K3_NS) * 32768 + aoff + (m) * 256)
#define K3_STEP(it_, ac, bc, an, bn, RD, ST) do { \
    if ((ST) && kh == 0) stage((it_) + 5); \
    _Pragma("unroll") for (int m = 0; m < 4; ++m) { \
      if (RD) { K3_RDB(bn, (it_) + 1, m); K3_RDA(an, (it_) + 1, m); } \
      _Pragma("unroll") for (int n = 0; n < 4; ++n) acc[m][n] = MFMA16(bc[n], ac[m], acc[m][n]); \
      SCHED; \
      if (m == 1 && (ST) && kh == 1) { stage((it_) + 5); SCHED; } } } while (0)
  STAMPR(2, 62); STAMP(2, 0);
  stage(0); stage(1); stage(2); stage(3); stage(4);
  WBAR(16);
#pragma unroll
  for (int n = 0; n < 4; ++n) { K3_RDB(b0, 0, n); K3_RDA(a0, 0, n); }
  STAMP(2, 1);
#pragma unroll
  for (int j = 0; j < 18; j += 2) {
    WBAR(12); K3_STEP(j, a0, b0, a1, b1, true, true);
    WBAR(12); K3_STEP(j + 1, a1, b1, a0, b0, true, true);
  }
  WBAR(12); K3_STEP(18, a0, b0, a1, b1, true, false);
  WBAR(8); K3_STEP(19, a1, b1, a0, b0, true, false);
  WBAR(4); K3_STEP(20, a0, b0, a1, b1, true, false);
  WBAR(0); K3_STEP(21, a1, b1, a0, b0, true, false);
  K3_STEP(22, a0, b0, a1, b1, false, false);
#undef K3_RDA
#undef K3_RDB
#undef K3_STEP
  f32x4 r2[2][4];
  {
    LAS char* mine = lds + wid * 8192 + lane * 16; LAS const char* theirs = lds + (wid ^ 4) * 8192 + lane * 16;
#pragma unroll
    for (int mm = 0; mm < 2; ++mm)
#pragma unroll
      for (int n = 0; n < 4; ++n) *(LAS f32x4*)(mine + (mm * 4 + n) * 1024) = kh ? acc[mm][n] : acc[2 + mm][n];
    LBAR();
#pragma unroll
    for (int mm = 0; mm < 2; ++mm)
#pragma unroll
      for (int n = 0; n < 4; ++n) { const f32x4 t = *(LAS const f32x4*)(theirs + (mm * 4 + n) * 1024); r2[mm][n] = kh ? t + acc[2 + mm][n] : acc[mm][n] + t; }
  }
  STAMP(2, 30);
  const int tile = rt * 4 + ct;
  const __amdgpu_buffer_rsrc_t rs = __builtin_amdgcn_make_buffer_rsrc((void*)(slabs + (size_t)tile * 12 * 2048), (short)0, 12 * 8192, 0x00020000);
  const int sl = wid * 1024 + lane * 16;
  f32x4 own[2] = {r2[0][0], r2[1][0]};
#pragma unroll
  for (int q = 0; q < 4; ++q) {
    if (q != sp) {
      u32x4 w = __builtin_bit_cast(u32x4, pack8(r2[0][q], r2[1][q], 1.0f));
#pragma unroll
      for (int e = 0; e < 4; ++e) w[e] = w[e] == SENT ? 0x7fc07fc0u : w[e];
      __builtin_amdgcn_raw_buffer_store_b128(w, rs, (q * 3 + (sp > q ? sp - 1 : sp)) * 8192 + sl, 0, 17);
    } else { own[0] = r2[0][q]; own[1] = r2[1][q]; }
  }
  STAMP(2, 31); STAMPR(2, 63);
  {
    const int col = ct * 128 + 64 * wc + 16 * sp + 4 * fq;
    const size_t roff = (size_t)(rt * 128 + 64 * wr + 32 * kh + fr) * DM + col;
    const u32x2 bb = *(const u32x2*)(b2 + col);
    u32x2 xx[2];
#pragma unroll
    for (int mm = 0; mm < 2; ++mm) xx[mm] = *(const u32x2*)(x + roff + (size_t)mm * 16 * DM);
    u32x4 t[3];
    for (int tries = 0; tries < (1 << 14); ++tries) {
      bool ok = true;
      asm volatile("" ::: "memory");
#pragma unroll
      for (int o = 0; o < 3; ++o) t[o] = __builtin_amdgcn_raw_buffer_load_b128(rs, (sp * 3 + o) * 8192 + sl, 0, 17);
#pragma unroll
      for (int o = 0; o < 3; ++o)
#pragma unroll
        for (int e = 0; e < 4; ++e) ok = ok && (t[o][e] != SENT);
      if (__all(ok)) break;
      __builtin_amdgcn_s_sleep(4);
    }
#pragma unroll
    for (int mm = 0; mm < 2; ++mm) {
      f32x4 tv[3];
#pragma unroll
      for (int o = 0; o < 3; ++o) tv[o] = (f32x4){bflo(t[o][2 * mm]), bfhi(t[o][2 * mm]), bflo(t[o][2 * mm + 1]), bfhi(t[o][2 * mm + 1])};
      const f32x4 p0 = sp == 0 ? own[mm] : tv[0], p1 = sp == 1 ? own[mm] : (sp < 1 ? tv[0] : tv[1]), p2 = sp == 2 ? own[mm] : (sp < 2 ? tv[1] : tv[2]), p3 = sp == 3 ? own[mm] : tv[2];
      const f32x4 a = ((p0 + p1) + p2) + p3;
      u32x2 w = {cvtpk(a[0] + bflo(xx[mm][0]) + bflo(bb[0]), a[1] + bfhi(xx[mm][0]) + bfhi(bb[0])), cvtpk(a[2] + bflo(xx[mm][1]) + bflo(bb[1]), a[3] + bfhi(xx[mm][1]) + bfhi(bb[1]))};
      *(u32x2*)(out + roff + (size_t)mm * 16 * DM) = w;
    }
  }
  STAMP(2, 32); STAMPR(2, 61);
}

extern "C" void kernel_launch(void* const* d_in, const int* in_sizes, int n_in, void* d_out, int out_size, void* d_ws, size_t ws_size,
                              hipStream_t stream) {
  const u16* x = (const u16*)d_in[0];
  const u16* ckv = (const u16*)d_in[1];
  const u16* wq = (const u16*)d_in[2];
  const u16* wuk = (const u16*)d_in[3];
  const u16* wuv = (const u16*)d_in[4];
  const u16* wo = (const u16*)d_in[5];
  const u16* w1 = (const u16*)d_in[7];
  const u16* b1 = (const u16*)d_in[8];
  const u16* w2 = (const u16*)d_in[9];
  const u16* b2 = (const u16*)d_in[10];
  char* ws = (char*)d_ws;
  u16* Kws = (u16*)(ws + OFF_K); u16* Vws = (u16*)(ws + OFF_V); u16* xn_t = (u16*)(ws + OFF_XN); u16* Abuf = (u16*)(ws + OFF_A);
  unsigned* slabs = (unsigned*)(ws + OFF_SLAB);
  (void)hipFuncSetAttribute((const void*)k1_kv, hipFuncAttributeMaxDynamicSharedMemorySize, K1_LDS);
  (void)hipFuncSetAttribute((const void*)k2_attn, hipFuncAttributeMaxDynamicSharedMemorySize, K2_LDS);
  (void)hipFuncSetAttribute((const void*)k3_out, hipFuncAttributeMaxDynamicSharedMemorySize, K3_LDS);
  k1_kv<<<256, 512, K1_LDS, stream>>>(x, ckv, wuk, wuv, Kws, Vws, xn_t);
  k2_attn<<<256, 512, K2_LDS, stream>>>(xn_t, wq, w1, b1, Kws, Vws, Abuf, slabs, wo, w2);
  k3_out<<<256, 512, K3_LDS, stream>>>(Abuf, wo, w2, x, b2, slabs, (u16*)d_out);

}
```

```cpp
#include <hip/hip_runtime.h>
#include <hip/hip_bf16.h>
#include <stdint.h>
#include <type_traits>

typedef unsigned short u16;
typedef short bf16x8 __attribute__((ext_vector_type(8)));
typedef short s16x4 __attribute__((ext_vector_type(4)));
typedef short v4i16_t __attribute__((ext_vector_type(4)));
typedef float f32x2 __attribute__((ext_vector_type(2)));
typedef float f32x4 __attribute__((ext_vector_type(4)));
typedef float f32x16 __attribute__((ext_vector_type(16)));
typedef unsigned u32x4 __attribute__((ext_vector_type(4)));
typedef unsigned u32x2 __attribute__((ext_vector_type(2)));
typedef __bf16 bf16x2_t __attribute__((ext_vector_type(2)));
#define LAS __attribute__((address_space(3)))
#define DI __device__ __forceinline__

constexpr int SEQ = 2048, DM = 512, NH = 32, HD = 128, FF = 1792, KTOT = NH * HD + FF, NKT = KTOT / 64;
constexpr size_t MiB = 1024 * 1024;
constexpr unsigned SENT = 0xffffffffu;
constexpr size_t OFF_K = 0, OFF_V = 16 * MiB, OFF_XN = 32 * MiB, OFF_A = 34 * MiB, OFF_SLAB = 58 * MiB, OFF_CNT = 74 * MiB;

DI unsigned cvtpk(float lo, float hi) { f32x2 v = {lo, hi}; bf16x2_t b = __builtin_convertvector(v, bf16x2_t); return __builtin_bit_cast(unsigned, b); }
DI float rbf(float x) { return __uint_as_float(cvtpk(x, x) & 0xffff0000u); }
DI float bflo(unsigned w) { return __uint_as_float(w << 16); }
DI float bfhi(unsigned w) { return __uint_as_float(w & 0xffff0000u); }
DI s16x4 vtr(LAS const char* p) { return __builtin_bit_cast(s16x4, __builtin_amdgcn_ds_read_tr16_b64_v4i16((LAS v4i16_t*)p)); }
DI bf16x8 vtr2(LAS const char* p0, LAS const char* p1) { s16x4 a = vtr(p0), b = vtr(p1); return __builtin_shufflevector(a, b, 0, 1, 2, 3, 4, 5, 6, 7); }
DI float max3f(float a, float b, float c) { float r; asm("v_max3_f32 %0, %1, %2, %3" : "=v"(r) : "v"(a), "v"(b), "v"(c)); return r; }
DI float max2f(float a, float b) { float r; asm("v_max_f32_e32 %0, %1, %2" : "=v"(r) : "v"(a), "v"(b)); return r; }
DI float fadd_s(float a, float b) { float r; asm("v_add_f32_e32 %0, %1, %2" : "=v"(r) : "v"(a), "v"(b)); return r; }
DI int rfl(int x) { return __builtin_amdgcn_readfirstlane(x); }
#define SCHED __builtin_amdgcn_sched_barrier(0)
#define WAIT_V(n) asm volatile("s_waitcnt vmcnt(" #n ")" ::: "memory")
#define WAIT_L(n) asm volatile("s_waitcnt lgkmcnt(" #n ")" ::: "memory")
#define BAR __builtin_amdgcn_s_barrier()
#define MFMA32(a, b, c) __builtin_amdgcn_mfma_f32_32x32x16_bf16((a), (b), (c), 0, 0, 0)
#define MFMA16(a, b, c) __builtin_amdgcn_mfma_f32_16x16x32_bf16((a), (b), (c), 0, 0, 0)
#define STAMP(kid, i) do {} while (0)
#define STAMPR(kid, i) do {} while (0)

DI void dma1k(const void* sbase, unsigned voff, unsigned ldsdst) {
  unsigned sv;
  asm volatile("s_mov_b32 %0, m0\n\ts_mov_b32 m0, %3\n\ts_nop 0\n\t"
               "global_load_lds_dwordx4 %1, %2\n\t"
               "s_mov_b32 m0, %0" : "=&s"(sv) : "v"(voff), "s"(sbase), "s"(ldsdst) : "memory");
}
DI void touch64(const void* p, unsigned ldsdump) {
  unsigned sv;
  asm volatile("s_mov_b32 %0, m0\n\ts_mov_b32 m0, %2\n\ts_nop 0\n\t"
               "global_load_lds_dword %1, off\n\t"
               "s_mov_b32 m0, %0" : "=&s"(sv) : "v"(p), "s"(ldsdump) : "memory");
}
DI void dma2k(const void* sbase, unsigned voff, unsigned ldsdst) {
  unsigned sv;
  asm volatile("s_mov_b32 %0, m0\n\ts_mov_b32 m0, %3\n\ts_nop 0\n\t"
               "global_load_lds_dwordx4 %1, %2\n\t"
               "global_load_lds_dwordx4 %1, %2 offset:1024\n\t"
               "s_mov_b32 m0, %0" : "=&s"(sv) : "v"(voff), "s"(sbase), "s"(ldsdst) : "memory");
}
DI void dma4k(const void* sbase, unsigned voff, unsigned ldsdst) {
  unsigned sv;
  asm volatile("s_mov_b32 %0, m0\n\ts_mov_b32 m0, %3\n\ts_nop 0\n\t"
               "global_load_lds_dwordx4 %1, %2\n\t"
               "global_load_lds_dwordx4 %1, %2 offset:1024\n\t"
               "global_load_lds_dwordx4 %1, %2 offset:2048\n\t"
               "global_load_lds_dwordx4 %1, %2 offset:3072\n\t"
               "s_mov_b32 m0, %0" : "=&s"(sv) : "v"(voff), "s"(sbase), "s"(ldsdst) : "memory");
}

constexpr int K1_HTB = 128 * 64 * 2, K1_LDS = 8 * K1_HTB;
DI int lds_byte(int r, int c) { const int st = (r >> 4) * 2 + (c >> 5), rr = r & 15, cc = c & 31, ob = rr * 64 + cc * 2; return st * 1024 + (ob ^ (((ob >> 9) & 1) << 5)); }
DI void stage_rc(int b, int& R, int& C) { const int st = b / 1024, sb = b % 1024, swz = sb ^ (((sb >> 9) & 1) << 5); R = (st >> 1) * 16 + swz / 64; C = (st & 1) * 32 + (swz % 64) / 2; }

template <bool ISV>
DI void k1_body(const u16* __restrict__ A, const u16* __restrict__ W, u16* __restrict__ out, int pm, int pn, LAS char* lds,
                const u16* __restrict__ xrow, u16* __restrict__ xn_t, int row, const char* __restrict__ warm0, const char* __restrict__ warm1) {
  const int tid = threadIdx.x, wid = rfl(tid >> 6), lane = tid & 63, wr = wid >> 2, wc = wid & 3, fr = lane & 15, fq = lane >> 4;
  STAMPR(0, 62); STAMP(0, 0);
  u32x4 xv;
  asm volatile("global_load_dwordx4 %0, %1, off" : "=v"(xv) : "v"(xrow + lane * 8) : "memory");
  unsigned voffA[2], voffB[2];
#pragma unroll
  for (int i = 0; i < 2; ++i) {
    int R, C; stage_rc(tid * 16 + i * 8192, R, C); voffA[i] = (unsigned)(R * DM + C) * 2u;
    const int b = tid * 16 + i * 8192, kb = b >> 11, nh = (b >> 10) & 1, kr = (b >> 7) & 7, c16 = (b >> 4) & 7;
    const int slot = (c16 >> 1) ^ (((kr >> 1) & 1) | ((kb & 1) << 1));
    voffB[i] = (unsigned)((8 * kb + kr) * 4096 + 64 * nh + 16 * slot + 8 * (c16 & 1)) * 2u;
  }
  const unsigned ldsw = (unsigned)wid * 1024u;
  const int aoff = lds_byte(wr * 64 + fr, fq * 8);
  const int boff0 = fq * 2048 + (wc >> 1) * 1024 + ((lane >> 2) & 3) * 128 + (((2 * (wc & 1)) ^ (((lane >> 3) & 1) | ((fq & 1) << 1))) * 32) + 8 * (lane & 3), boff1 = boff0 ^ 32;
  const char* cA = (const char*)A + (size_t)pm * 256 * DM * 2;
  const char* cB = (const char*)W + (size_t)pn * 256 * 2;
  constexpr size_t AK = 64 * 2, AH = (size_t)128 * DM * 2, BK_ = (size_t)64 * 4096 * 2, BH = 128 * 2;
#define SA(b, h) (((b) * 2 + (h)) * K1_HTB)
#define SB(b, h) ((4 + (b) * 2 + (h)) * K1_HTB)
#define STAGE(bufoff, gbase, voff) do { _Pragma("unroll") for (int _i = 0; _i < 2; ++_i) \
    __builtin_amdgcn_global_load_lds((const unsigned*)((const char*)(gbase) + (voff)[_i]), (LAS unsigned*)(lds + (bufoff) + ldsw + _i * 8192), 16, 0, 0); } while (0)
#define STA(b, h, kt) STAGE(SA(b, h), cA + (h) * AH + (kt) * AK, voffA)
#define STB(b, h, kt) STAGE(SB(b, h), cB + (h) * BH + (kt) * BK_, voffB)
#define LDA(dst, b, h) do { _Pragma("unroll") for (int m = 0; m < 4; ++m) _Pragma("unroll") for (int k = 0; k < 2; ++k) dst[m][k] = *(const LAS bf16x8*)(lds + SA(b, h) + aoff + m * 2048 + k * 1024); } while (0)
#define LDB(dst, b, h) do { _Pragma("unroll") for (int n = 0; n < 2; ++n) _Pragma("unroll") for (int k = 0; k < 2; ++k) \
    dst[n][k] = vtr2(lds + SB(b, h) + (n ? boff1 : boff0) + k * 8192, lds + SB(b, h) + (n ? boff1 : boff0) + k * 8192 + 512); } while (0)
#define MMA(ai, bj, At, Bt) do { __builtin_amdgcn_s_setprio(1); _Pragma("unroll") for (int m = 0; m < 4; ++m) _Pragma("unroll") for (int n = 0; n < 2; ++n) _Pragma("unroll") for (int k = 0; k < 2; ++k) \
    acc[ai][bj][m][n] = ISV ? MFMA16(At[m][k], Bt[n][k], acc[ai][bj][m][n]) : MFMA16(Bt[n][k], At[m][k], acc[ai][bj][m][n]); __builtin_amdgcn_s_setprio(0); } while (0)
  f32x4 acc[2][2][4][2];
#pragma unroll
  for (int a = 0; a < 2; ++a)
#pragma unroll
    for (int b = 0; b < 2; ++b)
#pragma unroll
      for (int m = 0; m < 4; ++m)
#pragma unroll
        for (int n = 0; n < 2; ++n) acc[a][b][m][n] = (f32x4){0.f, 0.f, 0.f, 0.f};
  bf16x8 At[4][2], B0[2][2], B1[2][2];
  constexpr int nt = DM / 64;
  STB(0, 0, 0); STA(0, 0, 0); STB(0, 1, 0); STA(0, 1, 0);
  touch64(warm0, (unsigned)(uintptr_t)lds + K1_LDS); touch64(warm1, (unsigned)(uintptr_t)lds + K1_LDS);
  if (wr == 1) BAR;
  asm volatile("s_waitcnt vmcnt(6)" : "+v"(xv) :: "memory");
  BAR;
  {
    float v[8];
#pragma unroll
    for (int i = 0; i < 4; ++i) { v[2 * i] = bflo(xv[i]); v[2 * i + 1] = bfhi(xv[i]); }
    float s = 0.f;
#pragma unroll
    for (int i = 0; i < 8; ++i) s += v[i];
#pragma unroll
    for (int o = 32; o >= 1; o >>= 1) s += __shfl_xor(s, o);
    const float mean = s * (1.0f / DM);
    float q = 0.f;
#pragma unroll
    for (int i = 0; i < 8; ++i) { const float d = v[i] - mean; q += d * d; }
#pragma unroll
    for (int o = 32; o >= 1; o >>= 1) q += __shfl_xor(q, o);
    const float mb = rbf(mean), vb = rbf(q * (1.0f / DM));
    const float den = rbf(sqrtf(rbf(vb + 1e-5f)));
    u32x4 o4;
#pragma unroll
    for (int i = 0; i < 4; ++i) o4[i] = cvtpk(rbf(v[2 * i] - mb) / den, rbf(v[2 * i + 1] - mb) / den);
    *(u32x4*)(xn_t + ((size_t)((row >> 4) * 16 + (lane >> 2)) * 64 + (lane & 3) * 16 + (row & 15)) * 8) = o4;
  }
  STB(1, 0, 1); STA(1, 0, 1); STB(1, 1, 1);
  WAIT_V(6); BAR;
  STAMP(0, 1);
  for (int t = 0; t < nt - 2; t += 2) {
    LDB(B0, 0, 0); SCHED; LDA(At, 0, 0); STA(1, 1, t + 1);
    WAIT_L(8); BAR; WAIT_L(0); MMA(0, 0, At, B0); BAR; SCHED;
    LDB(B1, 0, 1); STB(0, 0, t + 2);
    BAR; WAIT_L(0); MMA(0, 1, At, B1); BAR;
    LDA(At, 0, 1); STA(0, 0, t + 2);
    BAR; WAIT_L(0); MMA(1, 0, At, B0); BAR; SCHED;
    STB(0, 1, t + 2);
    WAIT_V(6); BAR; MMA(1, 1, At, B1); BAR;
    LDB(B0, 1, 0); SCHED; LDA(At, 1, 0); STA(0, 1, t + 2);
    WAIT_L(8); BAR; WAIT_L(0); MMA(0, 0, At, B0); BAR; SCHED;
    LDB(B1, 1, 1); STB(1, 0, t + 3);
    BAR; WAIT_L(0); MMA(0, 1, At, B1); BAR;
    LDA(At, 1, 1); STA(1, 0, t + 3);
    BAR; WAIT_L(0); MMA(1, 0, At, B0); BAR; SCHED;
    STB(1, 1, t + 3);
    WAIT_V(6); BAR; MMA(1, 1, At, B1); BAR;
  }
  { LDB(B0, 0, 0); WAIT_V(0); LDA(At, 0, 0); STA(1, 1, nt - 1);
    BAR; WAIT_L(0); MMA(0, 0, At, B0); BAR;
    LDB(B1, 0, 1); BAR; WAIT_L(0); MMA(0, 1, At, B1); BAR;
    LDA(At, 0, 1); WAIT_V(4); BAR; WAIT_L(0); MMA(1, 0, At, B0); MMA(1, 1, At, B1); BAR; }
  { LDB(B0, 1, 0); LDA(At, 1, 0); WAIT_V(2); BAR; WAIT_L(0); MMA(0, 0, At, B0); BAR;
    LDB(B1, 1, 1); WAIT_V(0); BAR; WAIT_L(0); MMA(0, 1, At, B1); BAR;
    LDA(At, 1, 1); BAR; WAIT_L(0); MMA(1, 0, At, B0); MMA(1, 1, At, B1); BAR; }
  if (wr == 0) BAR;
  STAMP(0, 2);
#pragma unroll
  for (int ai = 0; ai < 2; ++ai)
#pragma unroll
    for (int bj = 0; bj < 2; ++bj) {
      const int head = 2 * pn + bj, tile = 4 * pm + 2 * ai + wr;
      const __amdgpu_buffer_rsrc_t trs = __builtin_amdgcn_make_buffer_rsrc((void*)(out + (size_t)(head * 32 + tile) * 8192), (short)0, 16384, 0x00020000);
      if (!ISV) {
#pragma unroll
        for (int m = 0; m < 4; ++m) {
          const f32x4 a0 = acc[ai][bj][m][0], a1 = acc[ai][bj][m][1];
          u32x4 w = {cvtpk(a0[0], a0[1]), cvtpk(a0[2], a0[3]), cvtpk(a1[0], a1[1]), cvtpk(a1[2], a1[3])};
          __builtin_amdgcn_raw_buffer_store_b128(w, trs, ((4 * wc + fq) * 64 + 16 * m + fr) * 16, 0, 0);
        }
      } else {
#pragma unroll
        for (int mp = 0; mp < 2; ++mp)
#pragma unroll
          for (int n = 0; n < 2; ++n) {
            const f32x4 a0 = acc[ai][bj][2 * mp][n], a1 = acc[ai][bj][2 * mp + 1][n];
            u32x4 w = {cvtpk(a0[0], a0[1]), cvtpk(a0[2], a0[3]), cvtpk(a1[0], a1[1]), cvtpk(a1[2], a1[3])};
            __builtin_amdgcn_raw_buffer_store_b128(w, trs, ((4 * mp + fq) * 128 + 32 * wc + 16 * n + fr) * 16, 0, 0);
          }
      }
    }
  STAMP(0, 3); STAMPR(0, 63);
#undef SA
#undef SB
#undef STAGE
#undef STA
#undef STB
#undef LDA
#undef LDB
#undef MMA
}

__global__ __launch_bounds__(512, 2) void k1_kv(const u16* __restrict__ x, const u16* __restrict__ ckv, const u16* __restrict__ wuk, const u16* __restrict__ wuv,
                                                u16* __restrict__ Kws, u16* __restrict__ Vws, u16* __restrict__ xn_t,
                                                const u16* __restrict__ wq, const u16* __restrict__ w1, const u16* __restrict__ wo, const u16* __restrict__ w2) {
  extern __shared__ __attribute__((aligned(16))) char smem[];
  const int b = blockIdx.x, xcd = b & 7, idx = b >> 3;
  const int pnn = ((idx >> 4) << 4) + xcd * 2 + ((idx >> 3) & 1), pm = idx & 7;
  const int row = b * 8 + rfl(threadIdx.x >> 6);
  const int gid = b * 512 + threadIdx.x, l0 = gid < 94208 ? gid >> 1 : 47103, h0 = (gid & 1) * 64;
  const char* warm0 = (l0 < 32768 ? (const char*)wq + (size_t)l0 * 128 : (const char*)w1 + (size_t)(l0 - 32768) * 128) + h0;
  const char* warm1 = (l0 < 32768 ? (const char*)wo + (size_t)l0 * 128 : (const char*)w2 + (size_t)(l0 - 32768) * 128) + h0;
  if (pnn < 16) k1_body<false>(ckv, wuk, Kws, pm, pnn, (LAS char*)smem, x + (size_t)row * DM, xn_t, row, warm0, warm1);
  else          k1_body<true>(ckv, wuv, Vws, pm, pnn - 16, (LAS char*)smem, x + (size_t)row * DM, xn_t, row, warm0, warm1);
}

#define LSTAMP(k) do {} while (0)
constexpr int SLOT = 16384, K2_LDS = 10 * SLOT;
constexpr float QSCALE = 0.08838834764831845f * 1.4426950408889634f;
constexpr float THR = 32.f;
#define WBAR(N) do { asm volatile("s_waitcnt vmcnt(" #N ") lgkmcnt(0)\n\ts_barrier" ::: "memory"); SCHED; } while (0)
#define LBAR() do { asm volatile("s_waitcnt lgkmcnt(0)\n\ts_barrier" ::: "memory"); SCHED; } while (0)
#define VBAR(N) do { asm volatile("s_waitcnt vmcnt(" #N ")\n\ts_barrier" ::: "memory"); SCHED; } while (0)
DI float xg_max(float v) { v = fmaxf(v, __shfl_xor(v, 16)); return fmaxf(v, __shfl_xor(v, 32)); }
DI float xg_sum(float v) { v += __shfl_xor(v, 16); return v + __shfl_xor(v, 32); }
DI bf16x8 pack8(const f32x4& a, const f32x4& b, float sc) {
  return __builtin_bit_cast(bf16x8, (u32x4){cvtpk(a[0] * sc, a[1] * sc), cvtpk(a[2] * sc, a[3] * sc), cvtpk(b[0] * sc, b[1] * sc), cvtpk(b[2] * sc, b[3] * sc)});
}

__global__ __launch_bounds__(512, 2) void k2_attn(const u16* __restrict__ xn_t, const u16* __restrict__ Wq, const u16* __restrict__ W1, const u16* __restrict__ b1,
                                                  const u16* __restrict__ Kws, const u16* __restrict__ Vws, u16* __restrict__ Abuf, unsigned* __restrict__ slabs,
                                                  const u16* __restrict__ Wo, const u16* __restrict__ W2) {
  extern __shared__ __attribute__((aligned(16))) char smem[];
  LAS char* lds = (LAS char*)smem;
  const unsigned lds0 = (unsigned)(uintptr_t)lds;
  const int tid = threadIdx.x, lane = tid & 63, wid = rfl(tid >> 6), grp = wid >> 2, wq = wid & 3, c = lane & 15, g = lane >> 4;
  const int b = blockIdx.x, head = (b & 7) * 4 + (b >> 6), x = (b >> 3) & 7;
  const bool doF = head < 28;
  const int fh = doF ? head : 27;
  const int sw = 2 * x + 2;
  const int blk0 = grp ? 15 - x : x;
  const char* Kh = (const char*)Kws + (size_t)head * 32 * SLOT;
  const char* Vh = (const char*)Vws + (size_t)head * 32 * SLOT;
  const unsigned vlin = (unsigned)(wq * 4096 + lane * 16);
  const int tb0 = grp ? 15 - 2 * x : 0;
  const __amdgpu_buffer_rsrc_t arsrc = __builtin_amdgcn_make_buffer_rsrc((void*)Abuf, (short)0, 16 * NKT * 16384, 0x00020000);
  const int cvoff = (32 * wq + c) * 16 + g * 2048;

  bf16x8 qf[4][2];
  STAMPR(1, 62); STAMP(1, 0);
  auto prologue = [&](auto ftag) __attribute__((always_inline)) {
    constexpr bool DOF = decltype(ftag)::value;
    const char* xbase = (const char*)xn_t + (size_t)(blk0 * 8 + 2 * wq) * 16384;
    const unsigned xvo = lane * 16;
    const int dsl = 16 * (((lane >> 1) & 3) ^ (((lane >> 4) & 1) | ((wid & 1) << 1))) + 8 * (lane & 1);
    const unsigned voffq = (unsigned)((8 * wid + (lane >> 3)) * 4096 + head * 128 + dsl) * 2u;
    const unsigned voff1 = (unsigned)((8 * wid + (lane >> 3)) * FF + fh * 64 + dsl) * 2u;
    constexpr int CH = 24576;
    auto stage_w = [&](int ch) __attribute__((always_inline)) {
      const char* q0 = (const char*)Wq + (size_t)ch * 64 * 4096 * 2;
      const char* f0 = (const char*)W1 + (size_t)ch * 64 * FF * 2;
      const unsigned d0 = lds0 + (ch & 3) * CH + wid * 3072;
      dma1k(q0, voffq, d0); dma1k(q0 + 128, voffq, d0 + 1024); dma1k(f0, voff1, d0 + 2048);
    };
    int wb[4];
#pragma unroll
    for (int sl = 0; sl < 4; ++sl) wb[sl] = g * 3072 + ((lane >> 2) & 3) * 128 + ((sl ^ (((lane >> 3) & 1) | ((g & 1) << 1))) * 32) + 8 * (lane & 3);
#define WFRAG(ch, ks, t) vtr2(lds + ((ch) & 3) * CH + wb[(t) & 3] + (ks) * 12288 + ((t) >> 2) * 1024, lds + ((ch) & 3) * CH + wb[(t) & 3] + (ks) * 12288 + ((t) >> 2) * 1024 + 512)
    f32x4 aq[12][2];
#pragma unroll
    for (int t = 0; t < 12; ++t) { aq[t][0] = (f32x4){0.f, 0.f, 0.f, 0.f}; aq[t][1] = (f32x4){0.f, 0.f, 0.f, 0.f}; }
    bf16x8 xr[4][4];
#define XLOAD(ch) asm volatile("global_load_dwordx4 %0, %4, %5\n\tglobal_load_dwordx4 %1, %4, %5 offset:1024\n\tglobal_load_dwordx4 %2, %4, %6\n\tglobal_load_dwordx4 %3, %4, %6 offset:1024" \
      : "=&v"(xr[(ch) & 3][0]), "=&v"(xr[(ch) & 3][1]), "=&v"(xr[(ch) & 3][2]), "=&v"(xr[(ch) & 3][3]) : "v"(xvo), "s"(xbase + (ch) * 2048), "s"(xbase + 16384 + (ch) * 2048) : "memory")
#define CWAIT(ch, N) do { asm volatile("s_waitcnt vmcnt(" #N ") lgkmcnt(0)\n\ts_barrier" : "+v"(xr[(ch) & 3][0]), "+v"(xr[(ch) & 3][1]), "+v"(xr[(ch) & 3][2]), "+v"(xr[(ch) & 3][3]) :: "memory"); SCHED; } while (0)
    constexpr int WPD = 6;
    bf16x8 wa[24 + WPD];
    auto compute = [&](int ch, auto&& act) __attribute__((always_inline)) {
#pragma unroll
      for (int i = 0; i < 24; ++i) {
        const int ks = i / 12, t = i % 12, in = i + WPD, chn = in < 24 ? ch : ch + 1, ksn = (in % 24) / 12, tn = in % 12;
        if ((in < 24 || ch < 7) && (tn < 8 || DOF)) wa[in] = WFRAG(chn, ksn, tn);
        if (t < 8 || DOF) { aq[t][0] = MFMA16(wa[i], xr[ch & 3][ks], aq[t][0]); aq[t][1] = MFMA16(wa[i], xr[ch & 3][2 + ks], aq[t][1]); }
        if (i % 6 == 1) act(i / 6);
        SCHED;
      }
#pragma unroll
      for (int j = 0; j < WPD; ++j) wa[j] = wa[24 + j];
    };
    u32x2 bb[4];
    const u16* b1p = b1 + fh * 64; const int bvo = g * 8;
    XLOAD(0); stage_w(0); XLOAD(1); stage_w(1); XLOAD(2); stage_w(2);
    CWAIT(0, 14); STAMP(1, 1);
#pragma unroll
    for (int j = 0; j < WPD; ++j) wa[j] = WFRAG(0, 0, j);
    CWAIT(1, 7); compute(0, [&](int i) __attribute__((always_inline)) { if (i == 0) XLOAD(3); if (i == 1) stage_w(3); });
    CWAIT(2, 7); compute(1, [&](int i) __attribute__((always_inline)) { if (i == 0) XLOAD(4); if (i == 1) stage_w(4); });
    CWAIT(3, 7); compute(2, [&](int i) __attribute__((always_inline)) { if (i == 0) XLOAD(5); if (i == 1) stage_w(5); });
    CWAIT(4, 7); compute(3, [&](int i) __attribute__((always_inline)) { if (i == 0) XLOAD(6); if (i == 1) stage_w(6); });
    CWAIT(5, 7); compute(4, [&](int i) __attribute__((always_inline)) { if (i == 0) XLOAD(7); if (i == 1) stage_w(7); });
    CWAIT(6, 7);
    compute(5, [&](int i) __attribute__((always_inline)) {
      if (i == 0) asm volatile("global_load_dwordx2 %0, %4, %5\n\tglobal_load_dwordx2 %1, %4, %5 offset:32\n\tglobal_load_dwordx2 %2, %4, %5 offset:64\n\tglobal_load_dwordx2 %3, %4, %5 offset:96"
                               : "=&v"(bb[0]), "=&v"(bb[1]), "=&v"(bb[2]), "=&v"(bb[3]) : "v"(bvo), "s"(b1p) : "memory");
    });
    CWAIT(7, 4);
    compute(6, [&](int i) __attribute__((always_inline)) {
      if (i == 0) dma4k(Kh + (size_t)tb0 * SLOT, vlin, lds0 + (grp ? 6 : 9) * SLOT + wq * 4096);
      if (i == 1) dma4k(Vh + (size_t)tb0 * SLOT, vlin, lds0 + (grp ? 7 : 0) * SLOT + wq * 4096);
      if (i == 2) dma4k(Kh + (size_t)(tb0 + 1) * SLOT, vlin, lds0 + (grp ? 8 : 1) * SLOT + wq * 4096);
    });
    compute(7, [&](int) __attribute__((always_inline)) {});
    STAMP(1, 2);
    asm volatile("s_waitcnt vmcnt(8) lgkmcnt(0)\n\ts_barrier" : "+v"(bb[0]), "+v"(bb[1]), "+v"(bb[2]), "+v"(bb[3]) :: "memory"); SCHED;
#undef XLOAD
#undef CWAIT
#undef WFRAG
#pragma unroll
    for (int u = 0; u < 4; ++u) { qf[u][0] = pack8(aq[2 * u][0], aq[2 * u + 1][0], QSCALE); qf[u][1] = pack8(aq[2 * u][1], aq[2 * u + 1][1], QSCALE); }
    if (grp == 1) {
      LAS char* st = lds + 4 * SLOT + wq * 8192 + lane * 16;
#pragma unroll
      for (int i = 0; i < 8; ++i) *(LAS bf16x8*)(st + i * 1024) = qf[i >> 1][i & 1];
    }
    if constexpr (DOF) {
      const int sbase = (blk0 * NKT + 64 + head) * 16384;
#pragma unroll
      for (int v = 0; v < 2; ++v)
#pragma unroll
        for (int qt = 0; qt < 2; ++qt) {
          u32x4 w;
#pragma unroll
          for (int hf = 0; hf < 2; ++hf) {
            const f32x4 a = aq[8 + 2 * v + hf][qt]; const u32x2 bv = bb[2 * v + hf];
            float t0 = rbf(rbf(a[0]) + bflo(bv[0])), t1 = rbf(rbf(a[1]) + bfhi(bv[0])), t2 = rbf(rbf(a[2]) + bflo(bv[1])), t3 = rbf(rbf(a[3]) + bfhi(bv[1]));
            t0 = t0 > 0.f ? t0 : 0.f; t1 = t1 > 0.f ? t1 : 0.f; t2 = t2 > 0.f ? t2 : 0.f; t3 = t3 > 0.f ? t3 : 0.f;
            w[2 * hf] = cvtpk(t0 * t0, t1 * t1); w[2 * hf + 1] = cvtpk(t2 * t2, t3 * t3);
          }
          __builtin_amdgcn_raw_buffer_store_b128(w, arsrc, cvoff + sbase + v * 8192 + qt * 256, 0, 0);
        }
    }
  };
  if (doF) prologue(std::true_type{}); else prologue(std::false_type{});
  {
    const __amdgpu_buffer_rsrc_t frs = __builtin_amdgcn_make_buffer_rsrc((void*)(slabs + (size_t)b * 6144), (short)0, 24576, 0x00020000);
#pragma unroll
    for (int i = 0; i < 3; ++i) __builtin_amdgcn_raw_buffer_store_b128((u32x4){SENT, SENT, SENT, SENT}, frs, tid * 16 + i * 8192, 0, 17);
  }
  const int klane = g * 1024 + c * 16, vlane = g * 2048 + c * 16;
  const int kslot0 = (grp ? 6 : 9) * SLOT, kslot1 = (grp ? 8 : 1) * SLOT, vslot0 = (grp ? 7 : 0) * SLOT, vslot1 = (grp ? 3 : 2) * SLOT;
  f32x4 o[8][2], sc[4][2], negm[2];
#pragma unroll
  for (int i = 0; i < 8; ++i) { o[i][0] = (f32x4){0.f, 0.f, 0.f, 0.f}; o[i][1] = (f32x4){0.f, 0.f, 0.f, 0.f}; }
  negm[0] = (f32x4){0.f, 0.f, 0.f, 0.f}; negm[1] = (f32x4){0.f, 0.f, 0.f, 0.f};
  bf16x8 pf[2][2];
  float mref[2] = {0.f, 0.f};
  f32x4 lacc[2] = {(f32x4){0.f, 0.f, 0.f, 0.f}, (f32x4){0.f, 0.f, 0.f, 0.f}};
  bf16x8 ones = {0x3f80, 0x3f80, 0x3f80, 0x3f80, 0x3f80, 0x3f80, 0x3f80, 0x3f80};
  asm volatile("" : "+v"(ones));
  auto store_ctx = [&](int blk, float inv0, float inv1) __attribute__((always_inline)) {
    const int sbase = (blk * NKT + 2 * head) * 16384;
#pragma unroll
    for (int v = 0; v < 4; ++v)
#pragma unroll
      for (int qt = 0; qt < 2; ++qt)
        __builtin_amdgcn_raw_buffer_store_b128(__builtin_bit_cast(u32x4, pack8(o[2 * v][qt], o[2 * v + 1][qt], qt ? inv1 : inv0)), arsrc, cvoff + sbase + v * 8192 + qt * 256, 0, 0);
  };
  constexpr int PD = 6;
  bf16x8 fr_[32];
  const int swk = grp ? 99 : sw;
  int ktile = tb0 + 1, vtile = tb0;
  unsigned kvo = 0, vvo = 0;
#define FRD(i) (((i) < 16) ? *(LAS const bf16x8*)(kp + ((i) >> 2) * 4096 + ((i) & 3) * 256) : *(LAS const bf16x8*)(vp + (((i) - 16) >> 3) * 8192 + (((i) - 16) & 7) * 256))
#define SETM0(v) asm volatile("s_mov_b32 m0, %0\n\ts_nop 0" :: "s"(v) : "memory")
#define DMAI(vo, base, off) asm volatile("global_load_lds_dwordx4 %0, %1 offset:" #off :: "v"(vo), "s"(base) : "memory")
#define MBODY(i) do { if (i < 16) { sc[i & 3][0] = MFMA16(fr_[i], qf[i >> 2][0], i < 4 ? negm[0] : sc[i & 3][0]); sc[i & 3][1] = MFMA16(fr_[i], qf[i >> 2][1], i < 4 ? negm[1] : sc[i & 3][1]); } \
      else { o[(i - 16) & 7][0] = MFMA16(fr_[i], pf[(i - 16) >> 3][0], o[(i - 16) & 7][0]); o[(i - 16) & 7][1] = MFMA16(fr_[i], pf[(i - 16) >> 3][1], o[(i - 16) & 7][1]); \
        if (((i - 16) & 7) == 3) lacc[0] = MFMA16(ones, pf[(i - 16) >> 3][0], lacc[0]); if (((i - 16) & 7) == 5) lacc[1] = MFMA16(ones, pf[(i - 16) >> 3][1], lacc[1]); } } while (0)
#define KPIECE(s_, j) do { if ((j) == 0) { ktile = ((s_) + 2 == swk) ? 0 : ktile + 1; kvo = vlin + (unsigned)min(ktile, 31) * SLOT; SETM0(lds0 + (((s_) & 1) ? kslot1 : kslot0) + wq * 4096); DMAI(kvo, Kh, 0); } \
    if ((j) == 1) DMAI(kvo, Kh, 1024); if ((j) == 2) DMAI(kvo, Kh, 2048); if ((j) == 3) DMAI(kvo, Kh, 3072); } while (0)
#define VPIECE(s_, j) do { if ((j) == 0) { SETM0(lds0 + (((s_) & 1) ? vslot1 : vslot0) + wq * 4096); DMAI(vvo, Vh, 0); } \
    if ((j) == 1) DMAI(vvo, Vh, 1024); if ((j) == 2) DMAI(vvo, Vh, 2048); if ((j) == 3) DMAI(vvo, Vh, 3072); } while (0)
#define MPRE(s_, I0) do { \
    LAS const char* kp = lds + (((s_) & 1) ? kslot1 : kslot0) + klane; \
    LAS const char* vp = lds + (((s_) & 1) ? vslot0 : vslot1) + vlane; \
    _Pragma("unroll") for (int i = I0; i < I0 + PD; ++i) fr_[i] = FRD(i); \
    (void)kp; (void)vp; } while (0)
#define MSLOT(s_, I0, I1, DM) do { \
    LAS const char* kp = lds + (((s_) & 1) ? kslot1 : kslot0) + klane; \
    LAS const char* vp = lds + (((s_) & 1) ? vslot0 : vslot1) + vlane; \
    if (DM) { vtile = ((s_) == swk) ? 0 : vtile + 1; vvo = vlin + (unsigned)min(vtile, 31) * SLOT; } \
    _Pragma("unroll") for (int i = I0; i < I1; ++i) { \
      if (i + PD < I1) fr_[i + PD] = FRD(i + PD); \
      MBODY(i); \
      if ((DM) && (i & 3) == 2 && i < 16) VPIECE(s_, i >> 2); \
      SCHED; \
    } (void)kp; (void)vp; } while (0)
#define SCV(i) sc[((i) >> 2) & 3][(i) >> 4][(i) & 3]
  auto sslot = [&](const int s, const bool SLOW) __attribute__((always_inline)) {
    KPIECE(s, 0);
    bool first = false;
    if (__builtin_expect(SLOW, 0)) {
      const bool swst = (s == swk);
      if (swst) {
        store_ctx(x, 1.0f / lacc[0][0], 1.0f / lacc[1][0]);
#pragma unroll
        for (int i = 0; i < 8; ++i) { o[i][0] = (f32x4){0.f, 0.f, 0.f, 0.f}; o[i][1] = (f32x4){0.f, 0.f, 0.f, 0.f}; }
        lacc[0] = (f32x4){0.f, 0.f, 0.f, 0.f}; lacc[1] = (f32x4){0.f, 0.f, 0.f, 0.f};
      }
      first = (s == 0) || swst;
      const int jb = grp ? s - 15 : s - (sw - 2);
      if (jb >= 0 && jb <= 1) {
        const int dq = 32 * wq + c - 64 * jb - 4 * g;
#pragma unroll
        for (int i = 0; i < 32; ++i) if (16 * ((i >> 2) & 3) + (i & 3) > dq + 16 * (i >> 4)) SCV(i) = -__builtin_inff();
      }
    }
    float pm0 = max3f(SCV(0), SCV(1), SCV(2)), pm1 = max3f(SCV(16), SCV(17), SCV(18));
#pragma unroll
    for (int i = 3; i < 15; i += 2) { pm0 = max3f(pm0, SCV(i), SCV(i + 1)); pm1 = max3f(pm1, SCV(16 + i), SCV(17 + i)); }
    pm0 = max2f(pm0, SCV(15)); pm1 = max2f(pm1, SCV(31));
    KPIECE(s, 1);
    if (__builtin_expect(first || __any(max2f(pm0, pm1) > THR), 0)) {
      const float r0 = xg_max(pm0), r1 = xg_max(pm1);
      const float dl[2] = {first ? r0 : fmaxf(r0, 0.f), first ? r1 : fmaxf(r1, 0.f)};
#pragma unroll
      for (int qt = 0; qt < 2; ++qt) {
        mref[qt] += dl[qt];
#pragma unroll
        for (int kt = 0; kt < 4; ++kt) sc[kt][qt] -= dl[qt];
        if (!first) {
          const float al = __builtin_amdgcn_exp2f(-dl[qt]);
          lacc[qt] *= al;
#pragma unroll
          for (int d = 0; d < 8; ++d) o[d][qt] *= al;
        }
        negm[qt] = (f32x4){-mref[qt], -mref[qt], -mref[qt], -mref[qt]};
      }
    }
    {
      unsigned pw[16];
      SCV(0) = __builtin_amdgcn_exp2f(SCV(0)); SCV(1) = __builtin_amdgcn_exp2f(SCV(1)); SCHED;
#pragma unroll
      for (int i = 0; i < 32; i += 2) {
        if (i + 2 < 32) SCV(i + 2) = __builtin_amdgcn_exp2f(SCV(i + 2));
        pw[i >> 1] = cvtpk(SCV(i), SCV(i + 1));
        if (i + 3 < 32) SCV(i + 3) = __builtin_amdgcn_exp2f(SCV(i + 3));
        if (i == 8) KPIECE(s, 2);
        if (i == 20) KPIECE(s, 3);
        SCHED;
      }
#pragma unroll
      for (int k4 = 0; k4 < 4; ++k4) pf[k4 & 1][k4 >> 1] = __builtin_bit_cast(bf16x8, (u32x4){pw[4 * k4], pw[4 * k4 + 1], pw[4 * k4 + 2], pw[4 * k4 + 3]});
    }
    if (__builtin_expect(SLOW, 0)) {
      if (s + 1 == swk) {
        LAS const char* st = lds + 4 * SLOT + wq * 8192 + lane * 16;
#pragma unroll
        for (int i = 0; i < 8; ++i) qf[i >> 1][i & 1] = *(LAS const bf16x8*)(st + i * 1024);
        mref[0] = 0.f; mref[1] = 0.f; negm[0] = (f32x4){0.f, 0.f, 0.f, 0.f}; negm[1] = (f32x4){0.f, 0.f, 0.f, 0.f};
      }
    }
    asm volatile("" : "+v"(pf[0][0]), "+v"(pf[0][1]), "+v"(pf[1][0]), "+v"(pf[1][1]), "+v"(negm[0]), "+v"(negm[1]));
  };
  auto warm_k3 = [&]() __attribute__((always_inline)) {
#pragma unroll
    for (int i = 0; i < 3; ++i) {
      int sec = (wq * 3 + i) * 64 + lane; sec = sec < 736 ? sec : 735;
      const int kr = 1472 * ((b & 7) >> 1) + 46 * (b >> 3) + (sec >> 4);
      const char* src = (kr < 4096 ? (const char*)Wo + (size_t)kr * 1024 : (const char*)W2 + (size_t)(kr - 4096) * 1024) + (sec & 15) * 64;
      touch64(src, lds0 + 8 * SLOT + wq * 256);
    }
  };
  const unsigned spec = grp ? 0x18001u : (0x0001u | (7u << (sw - 2)));
  LAS char* pb = lds + (wq == 0 ? 9 : (wq == 1 ? 1 : 2 + wq)) * SLOT + lane * 16;
  LAS char* mlp = lds + 2 * SLOT + wq * 1024 + lane * 16;
  STAMP(1, 3);
  if (grp == 1) LBAR();
  MPRE(0, 0); SCHED;
  MSLOT(0, 0, 16, false);
  WBAR(0);
  for (int s = 0; s < 16; ++s) {
    LSTAMP(5);
    sslot(s, (spec >> s) & 1u);
    MPRE(s + 1, 0);
    LSTAMP(6);
    if (__builtin_expect(s == swk, 0)) VBAR(12); else VBAR(4);
    LSTAMP(7);
    MSLOT(s + 1, 0, 32, true);
    LSTAMP(8);
    WBAR(4);
  }
  STAMP(1, 37);
  sslot(16, true);
  WBAR(0);
  if (grp == 1) warm_k3();
  MPRE(17, 16); SCHED;
  MSLOT(17, 16, 32, false);
  STAMP(1, 40);
  if (grp == 0) {
#pragma unroll
    for (int i = 0; i < 16; ++i) *(LAS f32x4*)(pb + i * 1024) = o[i >> 1][i & 1];
    *(LAS f32x4*)mlp = (f32x4){mref[0], lacc[0][0], mref[1], lacc[1][0]};
    LBAR();
  } else {
    const f32x4 ml = *(LAS const f32x4*)mlp;
    float ga[2], gb[2];
#pragma unroll
    for (int qt = 0; qt < 2; ++qt) {
      const float mn = fmaxf(ml[2 * qt], mref[qt]), fa = __builtin_amdgcn_exp2f(ml[2 * qt] - mn), fb = __builtin_amdgcn_exp2f(mref[qt] - mn);
      const float inv = 1.0f / (lacc[qt][0] * fb + ml[2 * qt + 1] * fa);
      ga[qt] = fa * inv; gb[qt] = fb * inv;
    }
#pragma unroll
    for (int i = 0; i < 16; ++i) { const f32x4 pa = *(LAS const f32x4*)(pb + i * 1024); o[i >> 1][i & 1] = o[i >> 1][i & 1] * gb[i & 1] + pa * ga[i & 1]; }
    store_ctx(15 - x, 1.0f, 1.0f);
  }
#undef FRD
#undef MSLOT
#undef MPRE
#undef MBODY
#undef KPIECE
#undef VPIECE
#undef SCV
  STAMP(1, 41); STAMPR(1, 63);
}

constexpr int K3_NS = 5, K3_LDS = K3_NS * 32768;
__global__ __launch_bounds__(512, 2) void k3_out(const u16* __restrict__ Abuf, const u16* __restrict__ Wo, const u16* __restrict__ W2, const u16* __restrict__ x,
                                                 const u16* __restrict__ b2, unsigned* __restrict__ slabs, u16* __restrict__ out) {
  extern __shared__ __attribute__((aligned(16))) char smem[];
  LAS char* lds = (LAS char*)smem;
  const unsigned lds0 = (unsigned)(uintptr_t)lds;
  const int tid = threadIdx.x, lane = tid & 63, wid = rfl(tid >> 6), kh = wid >> 2, wr = (wid >> 1) & 1, wc = wid & 1, fr = lane & 15, fq = lane >> 4;
  const int b = blockIdx.x, xcd = b & 7, idx = b >> 3, sp = xcd >> 1, rt = (xcd & 1) * 8 + (idx >> 2), ct = idx & 3;
  const int kt0 = sp * 23;
  const char* Ab = (const char*)Abuf + (size_t)(rt * NKT + kt0) * 16384;
  const unsigned voffa = (unsigned)(wid * 2048 + lane * 16);
  const unsigned voffb = (unsigned)((32 * (wid >> 2) + 16 * (lane >> 5) + 4 * (wid & 3) + ((lane >> 3) & 3)) * DM + 128 * ct + 16 * (((lane >> 1) & 3) ^ (((lane >> 4) & 1) | ((wid & 1) << 1))) + 8 * (lane & 1)) * 2u;
  auto stage = [&](int it) __attribute__((always_inline)) {
    const int kt = kt0 + it;
    const char* wb = kt < 64 ? (const char*)Wo + (size_t)kt * 64 * DM * 2 : (const char*)W2 + (size_t)(kt - 64) * 64 * DM * 2;
    const unsigned d = lds0 + (it % K3_NS) * 32768 + wid * 2048;
    dma2k(Ab + (size_t)it * 16384, voffa, d);
    dma1k(wb, voffb, d + 16384); dma1k(wb + 128, voffb, d + 16384 + 1024);
  };
  const int aoff = (4 * kh + fq) * 2048 + (wr * 64 + fr) * 16;
  int boff[4];
#pragma unroll
  for (int n = 0; n < 4; ++n) boff[n] = 16384 + (4 * kh + fq) * 2048 + wc * 1024 + ((lane >> 2) & 3) * 128 + ((n ^ (((lane >> 3) & 1) | ((fq & 1) << 1))) * 32) + 8 * (lane & 3);
  f32x4 acc[4][4];
#pragma unroll
  for (int m = 0; m < 4; ++m)
#pragma unroll
    for (int n = 0; n < 4; ++n) acc[m][n] = (f32x4){0.f, 0.f, 0.f, 0.f};
  bf16x8 a0[4], b0[4], a1[4], b1[4];
#define K3_RDB(bf, it_, n) bf[n] = vtr2(lds + ((it_) % K3_NS) * 32768 + boff[n], lds + ((it_) % K3_NS) * 32768 + boff[n] + 512)
#define K3_RDA(af, it_, m) af[m] = *(LAS const bf16x8*)(lds + ((it_) % K3_NS) * 32768 + aoff + (m) * 256)
#define K3_STEP(it_, ac, bc, an, bn, RD, ST) do { \
    if ((ST) && kh == 0) stage((it_) + 5); \
    _Pragma("unroll") for (int m = 0; m < 4; ++m) { \
      if (RD) { K3_RDB(bn, (it_) + 1, m); K3_RDA(an, (it_) + 1, m); } \
      _Pragma("unroll") for (int n = 0; n < 4; ++n) acc[m][n] = MFMA16(bc[n], ac[m], acc[m][n]); \
      SCHED; \
      if (m == 1 && (ST) && kh == 1) { stage((it_) + 5); SCHED; } } } while (0)
  STAMPR(2, 62); STAMP(2, 0);
  stage(0); stage(1); stage(2); stage(3); stage(4);
  WBAR(16);
#pragma unroll
  for (int n = 0; n < 4; ++n) { K3_RDB(b0, 0, n); K3_RDA(a0, 0, n); }
  STAMP(2, 1);
#pragma unroll
  for (int j = 0; j < 18; j += 2) {
    WBAR(12); K3_STEP(j, a0, b0, a1, b1, true, true);
    WBAR(12); K3_STEP(j + 1, a1, b1, a0, b0, true, true);
  }
  WBAR(12); K3_STEP(18, a0, b0, a1, b1, true, false);
  WBAR(8); K3_STEP(19, a1, b1, a0, b0, true, false);
  WBAR(4); K3_STEP(20, a0, b0, a1, b1, true, false);
  WBAR(0); K3_STEP(21, a1, b1, a0, b0, true, false);
  K3_STEP(22, a0, b0, a1, b1, false, false);
#undef K3_RDA
#undef K3_RDB
#undef K3_STEP
  f32x4 r2[2][4];
  {
    LAS char* mine = lds + wid * 8192 + lane * 16; LAS const char* theirs = lds + (wid ^ 4) * 8192 + lane * 16;
#pragma unroll
    for (int mm = 0; mm < 2; ++mm)
#pragma unroll
      for (int n = 0; n < 4; ++n) *(LAS f32x4*)(mine + (mm * 4 + n) * 1024) = kh ? acc[mm][n] : acc[2 + mm][n];
    LBAR();
#pragma unroll
    for (int mm = 0; mm < 2; ++mm)
#pragma unroll
      for (int n = 0; n < 4; ++n) { const f32x4 t = *(LAS const f32x4*)(theirs + (mm * 4 + n) * 1024); r2[mm][n] = kh ? t + acc[2 + mm][n] : acc[mm][n] + t; }
  }
  STAMP(2, 30);
  const int tile = rt * 4 + ct;
  const __amdgpu_buffer_rsrc_t rs = __builtin_amdgcn_make_buffer_rsrc((void*)(slabs + (size_t)tile * 12 * 2048), (short)0, 12 * 8192, 0x00020000);
  const int sl = wid * 1024 + lane * 16;
  f32x4 own[2] = {r2[0][0], r2[1][0]};
#pragma unroll
  for (int q = 0; q < 4; ++q) {
    if (q != sp) {
      u32x4 w = __builtin_bit_cast(u32x4, pack8(r2[0][q], r2[1][q], 1.0f));
#pragma unroll
      for (int e = 0; e < 4; ++e) w[e] = w[e] == SENT ? 0x7fc07fc0u : w[e];
      __builtin_amdgcn_raw_buffer_store_b128(w, rs, (q * 3 + (sp > q ? sp - 1 : sp)) * 8192 + sl, 0, 17);
    } else { own[0] = r2[0][q]; own[1] = r2[1][q]; }
  }
  STAMP(2, 31); STAMPR(2, 63);
  {
    const int col = ct * 128 + 64 * wc + 16 * sp + 4 * fq;
    const size_t roff = (size_t)(rt * 128 + 64 * wr + 32 * kh + fr) * DM + col;
    const u32x2 bb = *(const u32x2*)(b2 + col);
    u32x2 xx[2];
#pragma unroll
    for (int mm = 0; mm < 2; ++mm) xx[mm] = *(const u32x2*)(x + roff + (size_t)mm * 16 * DM);
    u32x4 t[3];
    for (int tries = 0; tries < (1 << 14); ++tries) {
      bool ok = true;
      asm volatile("" ::: "memory");
#pragma unroll
      for (int o = 0; o < 3; ++o) t[o] = __builtin_amdgcn_raw_buffer_load_b128(rs, (sp * 3 + o) * 8192 + sl, 0, 17);
#pragma unroll
      for (int o = 0; o < 3; ++o)
#pragma unroll
        for (int e = 0; e < 4; ++e) ok = ok && (t[o][e] != SENT);
      if (__all(ok)) break;
      __builtin_amdgcn_s_sleep(4);
    }
#pragma unroll
    for (int mm = 0; mm < 2; ++mm) {
      f32x4 tv[3];
#pragma unroll
      for (int o = 0; o < 3; ++o) tv[o] = (f32x4){bflo(t[o][2 * mm]), bfhi(t[o][2 * mm]), bflo(t[o][2 * mm + 1]), bfhi(t[o][2 * mm + 1])};
      const f32x4 p0 = sp == 0 ? own[mm] : tv[0], p1 = sp == 1 ? own[mm] : (sp < 1 ? tv[0] : tv[1]), p2 = sp == 2 ? own[mm] : (sp < 2 ? tv[1] : tv[2]), p3 = sp == 3 ? own[mm] : tv[2];
      const f32x4 a = ((p0 + p1) + p2) + p3;
      u32x2 w = {cvtpk(a[0] + bflo(xx[mm][0]) + bflo(bb[0]), a[1] + bfhi(xx[mm][0]) + bfhi(bb[0])), cvtpk(a[2] + bflo(xx[mm][1]) + bflo(bb[1]), a[3] + bfhi(xx[mm][1]) + bfhi(bb[1]))};
      *(u32x2*)(out + roff + (size_t)mm * 16 * DM) = w;
    }
  }
  STAMP(2, 32); STAMPR(2, 61);
}

extern "C" void kernel_launch(void* const* d_in, const int* in_sizes, int n_in, void* d_out, int out_size, void* d_ws, size_t ws_size,
                              hipStream_t stream) {
  const u16* x = (const u16*)d_in[0];
  const u16* ckv = (const u16*)d_in[1];
  const u16* wq = (const u16*)d_in[2];
  const u16* wuk = (const u16*)d_in[3];
  const u16* wuv = (const u16*)d_in[4];
  const u16* wo = (const u16*)d_in[5];
  const u16* w1 = (const u16*)d_in[7];
  const u16* b1 = (const u16*)d_in[8];
  const u16* w2 = (const u16*)d_in[9];
  const u16* b2 = (const u16*)d_in[10];
  char* ws = (char*)d_ws;
  u16* Kws = (u16*)(ws + OFF_K); u16* Vws = (u16*)(ws + OFF_V); u16* xn_t = (u16*)(ws + OFF_XN); u16* Abuf = (u16*)(ws + OFF_A);
  unsigned* slabs = (unsigned*)(ws + OFF_SLAB);
  (void)hipFuncSetAttribute((const void*)k1_kv, hipFuncAttributeMaxDynamicSharedMemorySize, K1_LDS + 256);
  (void)hipFuncSetAttribute((const void*)k2_attn, hipFuncAttributeMaxDynamicSharedMemorySize, K2_LDS);
  (void)hipFuncSetAttribute((const void*)k3_out, hipFuncAttributeMaxDynamicSharedMemorySize, K3_LDS);
  k1_kv<<<256, 512, K1_LDS + 256, stream>>>(x, ckv, wuk, wuv, Kws, Vws, xn_t, wq, w1, wo, w2);
  k2_attn<<<256, 512, K2_LDS, stream>>>(xn_t, wq, w1, b1, Kws, Vws, Abuf, slabs, wo, w2);
  k3_out<<<256, 512, K3_LDS, stream>>>(Abuf, wo, w2, x, b2, slabs, (u16*)d_out);

}
```

```cpp
#include <hip/hip_runtime.h>
#include <hip/hip_bf16.h>
#include <stdint.h>
#include <type_traits>

typedef unsigned short u16;
typedef short bf16x8 __attribute__((ext_vector_type(8)));
typedef short s16x4 __attribute__((ext_vector_type(4)));
typedef short v4i16_t __attribute__((ext_vector_type(4)));
typedef float f32x2 __attribute__((ext_vector_type(2)));
typedef float f32x4 __attribute__((ext_vector_type(4)));
typedef float f32x16 __attribute__((ext_vector_type(16)));
typedef unsigned u32x4 __attribute__((ext_vector_type(4)));
typedef unsigned u32x2 __attribute__((ext_vector_type(2)));
typedef __bf16 bf16x2_t __attribute__((ext_vector_type(2)));
#define LAS __attribute__((address_space(3)))
#define DI __device__ __forceinline__

constexpr int SEQ = 2048, DM = 512, NH = 32, HD = 128, FF = 1792, KTOT = NH * HD + FF, NKT = KTOT / 64;
constexpr size_t MiB = 1024 * 1024;
constexpr unsigned SENT = 0xffffffffu;
constexpr size_t OFF_K = 0, OFF_V = 16 * MiB, OFF_XN = 32 * MiB, OFF_A = 34 * MiB, OFF_SLAB = 58 * MiB, OFF_CNT = 74 * MiB;

DI unsigned cvtpk(float lo, float hi) { f32x2 v = {lo, hi}; bf16x2_t b = __builtin_convertvector(v, bf16x2_t); return __builtin_bit_cast(unsigned, b); }
DI float rbf(float x) { return __uint_as_float(cvtpk(x, x) & 0xffff0000u); }
DI float bflo(unsigned w) { return __uint_as_float(w << 16); }
DI float bfhi(unsigned w) { return __uint_as_float(w & 0xffff0000u); }
DI s16x4 vtr(LAS const char* p) { return __builtin_bit_cast(s16x4, __builtin_amdgcn_ds_read_tr16_b64_v4i16((LAS v4i16_t*)p)); }
DI bf16x8 vtr2(LAS const char* p0, LAS const char* p1) { s16x4 a = vtr(p0), b = vtr(p1); return __builtin_shufflevector(a, b, 0, 1, 2, 3, 4, 5, 6, 7); }
DI float max3f(float a, float b, float c) { float r; asm("v_max3_f32 %0, %1, %2, %3" : "=v"(r) : "v"(a), "v"(b), "v"(c)); return r; }
DI float max2f(float a, float b) { float r; asm("v_max_f32_e32 %0, %1, %2" : "=v"(r) : "v"(a), "v"(b)); return r; }
DI float fadd_s(float a, float b) { float r; asm("v_add_f32_e32 %0, %1, %2" : "=v"(r) : "v"(a), "v"(b)); return r; }
DI int rfl(int x) { return __builtin_amdgcn_readfirstlane(x); }
#define SCHED __builtin_amdgcn_sched_barrier(0)
#define WAIT_V(n) asm volatile("s_waitcnt vmcnt(" #n ")" ::: "memory")
#define WAIT_L(n) asm volatile("s_waitcnt lgkmcnt(" #n ")" ::: "memory")
#define BAR __builtin_amdgcn_s_barrier()
#define MFMA32(a, b, c) __builtin_amdgcn_mfma_f32_32x32x16_bf16((a), (b), (c), 0, 0, 0)
#define MFMA16(a, b, c) __builtin_amdgcn_mfma_f32_16x16x32_bf16((a), (b), (c), 0, 0, 0)
#define STAMP(kid, i) do {} while (0)
#define STAMPR(kid, i) do {} while (0)

DI void dma1k(const void* sbase, unsigned voff, unsigned ldsdst) {
  unsigned sv;
  asm volatile("s_mov_b32 %0, m0\n\ts_mov_b32 m0, %3\n\ts_nop 0\n\t"
               "global_load_lds_dwordx4 %1, %2\n\t"
               "s_mov_b32 m0, %0" : "=&s"(sv) : "v"(voff), "s"(sbase), "s"(ldsdst) : "memory");
}
DI void touch64(const void* p, unsigned ldsdump) {
  unsigned sv;
  asm volatile("s_mov_b32 %0, m0\n\ts_mov_b32 m0, %2\n\ts_nop 0\n\t"
               "global_load_lds_dword %1, off\n\t"
               "s_mov_b32 m0, %0" : "=&s"(sv) : "v"(p), "s"(ldsdump) : "memory");
}
DI void dma2k(const void* sbase, unsigned voff, unsigned ldsdst) {
  unsigned sv;
  asm volatile("s_mov_b32 %0, m0\n\ts_mov_b32 m0, %3\n\ts_nop 0\n\t"
               "global_load_lds_dwordx4 %1, %2\n\t"
               "global_load_lds_dwordx4 %1, %2 offset:1024\n\t"
               "s_mov_b32 m0, %0" : "=&s"(sv) : "v"(voff), "s"(sbase), "s"(ldsdst) : "memory");
}
DI void dma4k(const void* sbase, unsigned voff, unsigned ldsdst) {
  unsigned sv;
  asm volatile("s_mov_b32 %0, m0\n\ts_mov_b32 m0, %3\n\ts_nop 0\n\t"
               "global_load_lds_dwordx4 %1, %2\n\t"
               "global_load_lds_dwordx4 %1, %2 offset:1024\n\t"
               "global_load_lds_dwordx4 %1, %2 offset:2048\n\t"
               "global_load_lds_dwordx4 %1, %2 offset:3072\n\t"
               "s_mov_b32 m0, %0" : "=&s"(sv) : "v"(voff), "s"(sbase), "s"(ldsdst) : "memory");
}

constexpr int K1_HTB = 128 * 64 * 2, K1_LDS = 8 * K1_HTB;
DI int lds_byte(int r, int c) { const int st = (r >> 4) * 2 + (c >> 5), rr = r & 15, cc = c & 31, ob = rr * 64 + cc * 2; return st * 1024 + (ob ^ (((ob >> 9) & 1) << 5)); }
DI void stage_rc(int b, int& R, int& C) { const int st = b / 1024, sb = b % 1024, swz = sb ^ (((sb >> 9) & 1) << 5); R = (st >> 1) * 16 + swz / 64; C = (st & 1) * 32 + (swz % 64) / 2; }

template <bool ISV>
DI void k1_body(const u16* __restrict__ A, const u16* __restrict__ W, u16* __restrict__ out, int pm, int pn, LAS char* lds,
                const u16* __restrict__ xrow, u16* __restrict__ xn_t, int row, const char* __restrict__ warm0, const char* __restrict__ warm1) {
  const int tid = threadIdx.x, wid = rfl(tid >> 6), lane = tid & 63, wr = wid >> 2, wc = wid & 3, fr = lane & 15, fq = lane >> 4;
  STAMPR(0, 62); STAMP(0, 0);
  u32x4 xv;
  asm volatile("global_load_dwordx4 %0, %1, off" : "=v"(xv) : "v"(xrow + lane * 8) : "memory");
  unsigned voffA[2], voffB[2];
#pragma unroll
  for (int i = 0; i < 2; ++i) {
    int R, C; stage_rc(tid * 16 + i * 8192, R, C); voffA[i] = (unsigned)(R * DM + C) * 2u;
    const int b = tid * 16 + i * 8192, kb = b >> 11, nh = (b >> 10) & 1, kr = (b >> 7) & 7, c16 = (b >> 4) & 7;
    const int slot = (c16 >> 1) ^ (((kr >> 1) & 1) | ((kb & 1) << 1));
    voffB[i] = (unsigned)((8 * kb + kr) * 4096 + 64 * nh + 16 * slot + 8 * (c16 & 1)) * 2u;
  }
  const unsigned ldsw = (unsigned)wid * 1024u;
  const int aoff = lds_byte(wr * 64 + fr, fq * 8);
  const int boff0 = fq * 2048 + (wc >> 1) * 1024 + ((lane >> 2) & 3) * 128 + (((2 * (wc & 1)) ^ (((lane >> 3) & 1) | ((fq & 1) << 1))) * 32) + 8 * (lane & 3), boff1 = boff0 ^ 32;
  const char* cA = (const char*)A + (size_t)pm * 256 * DM * 2;
  const char* cB = (const char*)W + (size_t)pn * 256 * 2;
  constexpr size_t AK = 64 * 2, AH = (size_t)128 * DM * 2, BK_ = (size_t)64 * 4096 * 2, BH = 128 * 2;
#define SA(b, h) (((b) * 2 + (h)) * K1_HTB)
#define SB(b, h) ((4 + (b) * 2 + (h)) * K1_HTB)
#define STAGE(bufoff, gbase, voff) do { _Pragma("unroll") for (int _i = 0; _i < 2; ++_i) \
    __builtin_amdgcn_global_load_lds((const unsigned*)((const char*)(gbase) + (voff)[_i]), (LAS unsigned*)(lds + (bufoff) + ldsw + _i * 8192), 16, 0, 0); } while (0)
#define STA(b, h, kt) STAGE(SA(b, h), cA + (h) * AH + (kt) * AK, voffA)
#define STB(b, h, kt) STAGE(SB(b, h), cB + (h) * BH + (kt) * BK_, voffB)
#define LDA(dst, b, h) do { _Pragma("unroll") for (int m = 0; m < 4; ++m) _Pragma("unroll") for (int k = 0; k < 2; ++k) dst[m][k] = *(const LAS bf16x8*)(lds + SA(b, h) + aoff + m * 2048 + k * 1024); } while (0)
#define LDB(dst, b, h) do { _Pragma("unroll") for (int n = 0; n < 2; ++n) _Pragma("unroll") for (int k = 0; k < 2; ++k) \
    dst[n][k] = vtr2(lds + SB(b, h) + (n ? boff1 : boff0) + k * 8192, lds + SB(b, h) + (n ? boff1 : boff0) + k * 8192 + 512); } while (0)
#define MMA(ai, bj, At, Bt) do { __builtin_amdgcn_s_setprio(1); _Pragma("unroll") for (int m = 0; m < 4; ++m) _Pragma("unroll") for (int n = 0; n < 2; ++n) _Pragma("unroll") for (int k = 0; k < 2; ++k) \
    acc[ai][bj][m][n] = ISV ? MFMA16(At[m][k], Bt[n][k], acc[ai][bj][m][n]) : MFMA16(Bt[n][k], At[m][k], acc[ai][bj][m][n]); __builtin_amdgcn_s_setprio(0); } while (0)
  f32x4 acc[2][2][4][2];
#pragma unroll
  for (int a = 0; a < 2; ++a)
#pragma unroll
    for (int b = 0; b < 2; ++b)
#pragma unroll
      for (int m = 0; m < 4; ++m)
#pragma unroll
        for (int n = 0; n < 2; ++n) acc[a][b][m][n] = (f32x4){0.f, 0.f, 0.f, 0.f};
  bf16x8 At[4][2], B0[2][2], B1[2][2];
  constexpr int nt = DM / 64;
  STB(0, 0, 0); STA(0, 0, 0); STB(0, 1, 0); STA(0, 1, 0);
  touch64(warm0, (unsigned)(uintptr_t)lds + K1_LDS); touch64(warm1, (unsigned)(uintptr_t)lds + K1_LDS);
  if (wr == 1) BAR;
  asm volatile("s_waitcnt vmcnt(6)" : "+v"(xv) :: "memory");
  BAR;
  {
    float v[8];
#pragma unroll
    for (int i = 0; i < 4; ++i) { v[2 * i] = bflo(xv[i]); v[2 * i + 1] = bfhi(xv[i]); }
    float s = 0.f;
#pragma unroll
    for (int i = 0; i < 8; ++i) s += v[i];
#pragma unroll
    for (int o = 32; o >= 1; o >>= 1) s += __shfl_xor(s, o);
    const float mean = s * (1.0f / DM);
    float q = 0.f;
#pragma unroll
    for (int i = 0; i < 8; ++i) { const float d = v[i] - mean; q += d * d; }
#pragma unroll
    for (int o = 32; o >= 1; o >>= 1) q += __shfl_xor(q, o);
    const float mb = rbf(mean), vb = rbf(q * (1.0f / DM));
    const float den = rbf(sqrtf(rbf(vb + 1e-5f)));
    u32x4 o4;
#pragma unroll
    for (int i = 0; i < 4; ++i) o4[i] = cvtpk(rbf(v[2 * i] - mb) / den, rbf(v[2 * i + 1] - mb) / den);
    *(u32x4*)(xn_t + ((size_t)((row >> 4) * 16 + (lane >> 2)) * 64 + (lane & 3) * 16 + (row & 15)) * 8) = o4;
  }
  STB(1, 0, 1); STA(1, 0, 1); STB(1, 1, 1);
  WAIT_V(6); BAR;
  STAMP(0, 1);
  for (int t = 0; t < nt - 2; t += 2) {
    LDB(B0, 0, 0); SCHED; LDA(At, 0, 0); STA(1, 1, t + 1);
    WAIT_L(8); BAR; WAIT_L(0); MMA(0, 0, At, B0); BAR; SCHED;
    LDB(B1, 0, 1); STB(0, 0, t + 2);
    BAR; WAIT_L(0); MMA(0, 1, At, B1); BAR;
    LDA(At, 0, 1); STA(0, 0, t + 2);
    BAR; WAIT_L(0); MMA(1, 0, At, B0); BAR; SCHED;
    STB(0, 1, t + 2);
    WAIT_V(6); BAR; MMA(1, 1, At, B1); BAR;
    LDB(B0, 1, 0); SCHED; LDA(At, 1, 0); STA(0, 1, t + 2);
    WAIT_L(8); BAR; WAIT_L(0); MMA(0, 0, At, B0); BAR; SCHED;
    LDB(B1, 1, 1); STB(1, 0, t + 3);
    BAR; WAIT_L(0); MMA(0, 1, At, B1); BAR;
    LDA(At, 1, 1); STA(1, 0, t + 3);
    BAR; WAIT_L(0); MMA(1, 0, At, B0); BAR; SCHED;
    STB(1, 1, t + 3);
    WAIT_V(6); BAR; MMA(1, 1, At, B1); BAR;
  }
  { LDB(B0, 0, 0); WAIT_V(0); LDA(At, 0, 0); STA(1, 1, nt - 1);
    BAR; WAIT_L(0); MMA(0, 0, At, B0); BAR;
    LDB(B1, 0, 1); BAR; WAIT_L(0); MMA(0, 1, At, B1); BAR;
    LDA(At, 0, 1); WAIT_V(4); BAR; WAIT_L(0); MMA(1, 0, At, B0); MMA(1, 1, At, B1); BAR; }
  auto epi = [&](const int ai, const int bj) __attribute__((always_inline)) {
    const int head = 2 * pn + bj, tile = 4 * pm + 2 * ai + wr;
    const __amdgpu_buffer_rsrc_t trs = __builtin_amdgcn_make_buffer_rsrc((void*)(out + (size_t)(head * 32 + tile) * 8192), (short)0, 16384, 0x00020000);
    if (!ISV) {
#pragma unroll
      for (int m = 0; m < 4; ++m) {
        const f32x4 a0 = acc[ai][bj][m][0], a1 = acc[ai][bj][m][1];
        u32x4 w = {cvtpk(a0[0], a0[1]), cvtpk(a0[2], a0[3]), cvtpk(a1[0], a1[1]), cvtpk(a1[2], a1[3])};
        __builtin_amdgcn_raw_buffer_store_b128(w, trs, ((4 * wc + fq) * 64 + 16 * m + fr) * 16, 0, 0);
      }
    } else {
#pragma unroll
      for (int mp = 0; mp < 2; ++mp)
#pragma unroll
        for (int n = 0; n < 2; ++n) {
          const f32x4 a0 = acc[ai][bj][2 * mp][n], a1 = acc[ai][bj][2 * mp + 1][n];
          u32x4 w = {cvtpk(a0[0], a0[1]), cvtpk(a0[2], a0[3]), cvtpk(a1[0], a1[1]), cvtpk(a1[2], a1[3])};
          __builtin_amdgcn_raw_buffer_store_b128(w, trs, ((4 * mp + fq) * 128 + 32 * wc + 16 * n + fr) * 16, 0, 0);
        }
    }
  };
  { LDB(B0, 1, 0); LDA(At, 1, 0); WAIT_V(2); BAR; WAIT_L(0); MMA(0, 0, At, B0); BAR;
    LDB(B1, 1, 1); WAIT_V(0); BAR; WAIT_L(0); MMA(0, 1, At, B1); BAR;
    LDA(At, 1, 1); SCHED; epi(0, 0); SCHED; BAR; WAIT_L(0); MMA(1, 0, At, B0); MMA(1, 1, At, B1); BAR; }
  STAMP(0, 2);
  epi(0, 1); epi(1, 0); epi(1, 1);
  if (wr == 0) BAR;
  STAMP(0, 3); STAMPR(0, 63);
#undef SA
#undef SB
#undef STAGE
#undef STA
#undef STB
#undef LDA
#undef LDB
#undef MMA
}

__global__ __launch_bounds__(512, 2) void k1_kv(const u16* __restrict__ x, const u16* __restrict__ ckv, const u16* __restrict__ wuk, const u16* __restrict__ wuv,
                                                u16* __restrict__ Kws, u16* __restrict__ Vws, u16* __restrict__ xn_t,
                                                const u16* __restrict__ wq, const u16* __restrict__ w1, const u16* __restrict__ wo, const u16* __restrict__ w2) {
  extern __shared__ __attribute__((aligned(16))) char smem[];
  const int b = blockIdx.x, xcd = b & 7, idx = b >> 3;
  const int pnn = ((idx >> 4) << 4) + xcd * 2 + ((idx >> 3) & 1), pm = idx & 7;
  const int row = b * 8 + rfl(threadIdx.x >> 6);
  const int gid = b * 512 + threadIdx.x, l0 = gid < 94208 ? gid >> 1 : 47103, h0 = (gid & 1) * 64;
  const char* warm0 = (l0 < 32768 ? (const char*)wq + (size_t)l0 * 128 : (const char*)w1 + (size_t)(l0 - 32768) * 128) + h0;
  const char* warm1 = (l0 < 32768 ? (const char*)wo + (size_t)l0 * 128 : (const char*)w2 + (size_t)(l0 - 32768) * 128) + h0;
  if (pnn < 16) k1_body<false>(ckv, wuk, Kws, pm, pnn, (LAS char*)smem, x + (size_t)row * DM, xn_t, row, warm0, warm1);
  else          k1_body<true>(ckv, wuv, Vws, pm, pnn - 16, (LAS char*)smem, x + (size_t)row * DM, xn_t, row, warm0, warm1);
}

#define LSTAMP(k) do {} while (0)
constexpr int SLOT = 16384, K2_LDS = 10 * SLOT;
constexpr float QSCALE = 0.08838834764831845f * 1.4426950408889634f;
constexpr float THR = 32.f;
#define WBAR(N) do { asm volatile("s_waitcnt vmcnt(" #N ") lgkmcnt(0)\n\ts_barrier" ::: "memory"); SCHED; } while (0)
#define LBAR() do { asm volatile("s_waitcnt lgkmcnt(0)\n\ts_barrier" ::: "memory"); SCHED; } while (0)
#define VBAR(N) do { asm volatile("s_waitcnt vmcnt(" #N ")\n\ts_barrier" ::: "memory"); SCHED; } while (0)
DI float xg_max(float v) { v = fmaxf(v, __shfl_xor(v, 16)); return fmaxf(v, __shfl_xor(v, 32)); }
DI float xg_sum(float v) { v += __shfl_xor(v, 16); return v + __shfl_xor(v, 32); }
DI bf16x8 pack8(const f32x4& a, const f32x4& b, float sc) {
  return __builtin_bit_cast(bf16x8, (u32x4){cvtpk(a[0] * sc, a[1] * sc), cvtpk(a[2] * sc, a[3] * sc), cvtpk(b[0] * sc, b[1] * sc), cvtpk(b[2] * sc, b[3] * sc)});
}

__global__ __launch_bounds__(512, 2) void k2_attn(const u16* __restrict__ xn_t, const u16* __restrict__ Wq, const u16* __restrict__ W1, const u16* __restrict__ b1,
                                                  const u16* __restrict__ Kws, const u16* __restrict__ Vws, u16* __restrict__ Abuf, unsigned* __restrict__ slabs,
                                                  const u16* __restrict__ Wo, const u16* __restrict__ W2) {
  extern __shared__ __attribute__((aligned(16))) char smem[];
  LAS char* lds = (LAS char*)smem;
  const unsigned lds0 = (unsigned)(uintptr_t)lds;
  const int tid = threadIdx.x, lane = tid & 63, wid = rfl(tid >> 6), grp = wid >> 2, wq = wid & 3, c = lane & 15, g = lane >> 4;
  const int b = blockIdx.x, head = (b & 7) * 4 + (b >> 6), x = (b >> 3) & 7;
  const bool doF = head < 28;
  const int fh = doF ? head : 27;
  const int sw = 2 * x + 2;
  const int blk0 = grp ? 15 - x : x;
  const char* Kh = (const char*)Kws + (size_t)head * 32 * SLOT;
  const char* Vh = (const char*)Vws + (size_t)head * 32 * SLOT;
  const unsigned vlin = (unsigned)(wq * 4096 + lane * 16);
  const int tb0 = grp ? 15 - 2 * x : 0;
  const __amdgpu_buffer_rsrc_t arsrc = __builtin_amdgcn_make_buffer_rsrc((void*)Abuf, (short)0, 16 * NKT * 16384, 0x00020000);
  const int cvoff = (32 * wq + c) * 16 + g * 2048;

  bf16x8 qf[4][2];
  STAMPR(1, 62); STAMP(1, 0);
  auto prologue = [&](auto ftag) __attribute__((always_inline)) {
    constexpr bool DOF = decltype(ftag)::value;
    const char* xbase = (const char*)xn_t + (size_t)(blk0 * 8 + 2 * wq) * 16384;
    const unsigned xvo = lane * 16;
    const int dsl = 16 * (((lane >> 1) & 3) ^ (((lane >> 4) & 1) | ((wid & 1) << 1))) + 8 * (lane & 1);
    const unsigned voffq = (unsigned)((8 * wid + (lane >> 3)) * 4096 + head * 128 + dsl) * 2u;
    const unsigned voff1 = (unsigned)((8 * wid + (lane >> 3)) * FF + fh * 64 + dsl) * 2u;
    constexpr int CH = 24576;
    auto stage_w = [&](int ch) __attribute__((always_inline)) {
      const char* q0 = (const char*)Wq + (size_t)ch * 64 * 4096 * 2;
      const char* f0 = (const char*)W1 + (size_t)ch * 64 * FF * 2;
      const unsigned d0 = lds0 + (ch & 3) * CH + wid * 3072;
      dma1k(q0, voffq, d0); dma1k(q0 + 128, voffq, d0 + 1024); dma1k(f0, voff1, d0 + 2048);
    };
    int wb[4];
#pragma unroll
    for (int sl = 0; sl < 4; ++sl) wb[sl] = g * 3072 + ((lane >> 2) & 3) * 128 + ((sl ^ (((lane >> 3) & 1) | ((g & 1) << 1))) * 32) + 8 * (lane & 3);
#define WFRAG(ch, ks, t) vtr2(lds + ((ch) & 3) * CH + wb[(t) & 3] + (ks) * 12288 + ((t) >> 2) * 1024, lds + ((ch) & 3) * CH + wb[(t) & 3] + (ks) * 12288 + ((t) >> 2) * 1024 + 512)
    f32x4 aq[12][2];
#pragma unroll
    for (int t = 0; t < 12; ++t) { aq[t][0] = (f32x4){0.f, 0.f, 0.f, 0.f}; aq[t][1] = (f32x4){0.f, 0.f, 0.f, 0.f}; }
    bf16x8 xr[4][4];
#define XLOAD(ch) asm volatile("global_load_dwordx4 %0, %4, %5\n\tglobal_load_dwordx4 %1, %4, %5 offset:1024\n\tglobal_load_dwordx4 %2, %4, %6\n\tglobal_load_dwordx4 %3, %4, %6 offset:1024" \
      : "=&v"(xr[(ch) & 3][0]), "=&v"(xr[(ch) & 3][1]), "=&v"(xr[(ch) & 3][2]), "=&v"(xr[(ch) & 3][3]) : "v"(xvo), "s"(xbase + (ch) * 2048), "s"(xbase + 16384 + (ch) * 2048) : "memory")
#define CWAIT(ch, N) do { asm volatile("s_waitcnt vmcnt(" #N ") lgkmcnt(0)\n\ts_barrier" : "+v"(xr[(ch) & 3][0]), "+v"(xr[(ch) & 3][1]), "+v"(xr[(ch) & 3][2]), "+v"(xr[(ch) & 3][3]) :: "memory"); SCHED; } while (0)
    constexpr int WPD = 6;
    bf16x8 wa[24 + WPD];
    auto compute = [&](int ch, auto&& act) __attribute__((always_inline)) {
#pragma unroll
      for (int i = 0; i < 24; ++i) {
        const int ks = i / 12, t = i % 12, in = i + WPD, chn = in < 24 ? ch : ch + 1, ksn = (in % 24) / 12, tn = in % 12;
        if ((in < 24 || ch < 7) && (tn < 8 || DOF)) wa[in] = WFRAG(chn, ksn, tn);
        if (t < 8 || DOF) { aq[t][0] = MFMA16(wa[i], xr[ch & 3][ks], aq[t][0]); aq[t][1] = MFMA16(wa[i], xr[ch & 3][2 + ks], aq[t][1]); }
        if (i % 6 == 1) act(i / 6);
        SCHED;
      }
#pragma unroll
      for (int j = 0; j < WPD; ++j) wa[j] = wa[24 + j];
    };
    u32x2 bb[4];
    const u16* b1p = b1 + fh * 64; const int bvo = g * 8;
    XLOAD(0); stage_w(0); XLOAD(1); stage_w(1); XLOAD(2); stage_w(2);
    CWAIT(0, 14); STAMP(1, 1);
#pragma unroll
    for (int j = 0; j < WPD; ++j) wa[j] = WFRAG(0, 0, j);
    CWAIT(1, 7); compute(0, [&](int i) __attribute__((always_inline)) { if (i == 0) XLOAD(3); if (i == 1) stage_w(3); });
    CWAIT(2, 7); compute(1, [&](int i) __attribute__((always_inline)) { if (i == 0) XLOAD(4); if (i == 1) stage_w(4); });
    CWAIT(3, 7); compute(2, [&](int i) __attribute__((always_inline)) { if (i == 0) XLOAD(5); if (i == 1) stage_w(5); });
    CWAIT(4, 7); compute(3, [&](int i) __attribute__((always_inline)) { if (i == 0) XLOAD(6); if (i == 1) stage_w(6); });
    CWAIT(5, 7); compute(4, [&](int i) __attribute__((always_inline)) { if (i == 0) XLOAD(7); if (i == 1) stage_w(7); });
    CWAIT(6, 7);
    compute(5, [&](int i) __attribute__((always_inline)) {
      if (i == 0) asm volatile("global_load_dwordx2 %0, %4, %5\n\tglobal_load_dwordx2 %1, %4, %5 offset:32\n\tglobal_load_dwordx2 %2, %4, %5 offset:64\n\tglobal_load_dwordx2 %3, %4, %5 offset:96"
                               : "=&v"(bb[0]), "=&v"(bb[1]), "=&v"(bb[2]), "=&v"(bb[3]) : "v"(bvo), "s"(b1p) : "memory");
    });
    CWAIT(7, 4);
    compute(6, [&](int i) __attribute__((always_inline)) {
      if (i == 0) dma4k(Kh + (size_t)tb0 * SLOT, vlin, lds0 + (grp ? 6 : 9) * SLOT + wq * 4096);
      if (i == 1) dma4k(Vh + (size_t)tb0 * SLOT, vlin, lds0 + (grp ? 7 : 0) * SLOT + wq * 4096);
      if (i == 2) dma4k(Kh + (size_t)(tb0 + 1) * SLOT, vlin, lds0 + (grp ? 8 : 1) * SLOT + wq * 4096);
    });
    compute(7, [&](int) __attribute__((always_inline)) {});
    STAMP(1, 2);
    asm volatile("s_waitcnt vmcnt(8) lgkmcnt(0)\n\ts_barrier" : "+v"(bb[0]), "+v"(bb[1]), "+v"(bb[2]), "+v"(bb[3]) :: "memory"); SCHED;
#undef XLOAD
#undef CWAIT
#undef WFRAG
#pragma unroll
    for (int u = 0; u < 4; ++u) { qf[u][0] = pack8(aq[2 * u][0], aq[2 * u + 1][0], QSCALE); qf[u][1] = pack8(aq[2 * u][1], aq[2 * u + 1][1], QSCALE); }
    if (grp == 1) {
      LAS char* st = lds + 4 * SLOT + wq * 8192 + lane * 16;
#pragma unroll
      for (int i = 0; i < 8; ++i) *(LAS bf16x8*)(st + i * 1024) = qf[i >> 1][i & 1];
    }
    if constexpr (DOF) {
      const int sbase = (blk0 * NKT + 64 + head) * 16384;
#pragma unroll
      for (int v = 0; v < 2; ++v)
#pragma unroll
        for (int qt = 0; qt < 2; ++qt) {
          u32x4 w;
#pragma unroll
          for (int hf = 0; hf < 2; ++hf) {
            const f32x4 a = aq[8 + 2 * v + hf][qt]; const u32x2 bv = bb[2 * v + hf];
            float t0 = rbf(rbf(a[0]) + bflo(bv[0])), t1 = rbf(rbf(a[1]) + bfhi(bv[0])), t2 = rbf(rbf(a[2]) + bflo(bv[1])), t3 = rbf(rbf(a[3]) + bfhi(bv[1]));
            t0 = t0 > 0.f ? t0 : 0.f; t1 = t1 > 0.f ? t1 : 0.f; t2 = t2 > 0.f ? t2 : 0.f; t3 = t3 > 0.f ? t3 : 0.f;
            w[2 * hf] = cvtpk(t0 * t0, t1 * t1); w[2 * hf + 1] = cvtpk(t2 * t2, t3 * t3);
          }
          __builtin_amdgcn_raw_buffer_store_b128(w, arsrc, cvoff + sbase + v * 8192 + qt * 256, 0, 0);
        }
    }
  };
  if (doF) prologue(std::true_type{}); else prologue(std::false_type{});
  {
    const __amdgpu_buffer_rsrc_t frs = __builtin_amdgcn_make_buffer_rsrc((void*)(slabs + (size_t)b * 6144), (short)0, 24576, 0x00020000);
#pragma unroll
    for (int i = 0; i < 3; ++i) __builtin_amdgcn_raw_buffer_store_b128((u32x4){SENT, SENT, SENT, SENT}, frs, tid * 16 + i * 8192, 0, 17);
  }
  const int klane = g * 1024 + c * 16, vlane = g * 2048 + c * 16;
  const int kslot0 = (grp ? 6 : 9) * SLOT, kslot1 = (grp ? 8 : 1) * SLOT, vslot0 = (grp ? 7 : 0) * SLOT, vslot1 = (grp ? 3 : 2) * SLOT;
  f32x4 o[8][2], sc[4][2], negm[2];
#pragma unroll
  for (int i = 0; i < 8; ++i) { o[i][0] = (f32x4){0.f, 0.f, 0.f, 0.f}; o[i][1] = (f32x4){0.f, 0.f, 0.f, 0.f}; }
  negm[0] = (f32x4){0.f, 0.f, 0.f, 0.f}; negm[1] = (f32x4){0.f, 0.f, 0.f, 0.f};
  bf16x8 pf[2][2];
  float mref[2] = {0.f, 0.f};
  f32x4 lacc[2] = {(f32x4){0.f, 0.f, 0.f, 0.f}, (f32x4){0.f, 0.f, 0.f, 0.f}};
  bf16x8 ones = {0x3f80, 0x3f80, 0x3f80, 0x3f80, 0x3f80, 0x3f80, 0x3f80, 0x3f80};
  asm volatile("" : "+v"(ones));
  auto store_ctx = [&](int blk, float inv0, float inv1) __attribute__((always_inline)) {
    const int sbase = (blk * NKT + 2 * head) * 16384;
#pragma unroll
    for (int v = 0; v < 4; ++v)
#pragma unroll
      for (int qt = 0; qt < 2; ++qt)
        __builtin_amdgcn_raw_buffer_store_b128(__builtin_bit_cast(u32x4, pack8(o[2 * v][qt], o[2 * v + 1][qt], qt ? inv1 : inv0)), arsrc, cvoff + sbase + v * 8192 + qt * 256, 0, 0);
  };
  constexpr int PD = 6;
  bf16x8 fr_[32];
  const int swk = grp ? 99 : sw;
  int ktile = tb0 + 1, vtile = tb0;
  unsigned kvo = 0, vvo = 0;
#define FRD(i) (((i) < 16) ? *(LAS const bf16x8*)(kp + ((i) >> 2) * 4096 + ((i) & 3) * 256) : *(LAS const bf16x8*)(vp + (((i) - 16) >> 3) * 8192 + (((i) - 16) & 7) * 256))
#define SETM0(v) asm volatile("s_mov_b32 m0, %0\n\ts_nop 0" :: "s"(v) : "memory")
#define DMAI(vo, base, off) asm volatile("global_load_lds_dwordx4 %0, %1 offset:" #off :: "v"(vo), "s"(base) : "memory")
#define MBODY(i) do { if (i < 16) { sc[i & 3][0] = MFMA16(fr_[i], qf[i >> 2][0], i < 4 ? negm[0] : sc[i & 3][0]); sc[i & 3][1] = MFMA16(fr_[i], qf[i >> 2][1], i < 4 ? negm[1] : sc[i & 3][1]); } \
      else { o[(i - 16) & 7][0] = MFMA16(fr_[i], pf[(i - 16) >> 3][0], o[(i - 16) & 7][0]); o[(i - 16) & 7][1] = MFMA16(fr_[i], pf[(i - 16) >> 3][1], o[(i - 16) & 7][1]); \
        if (((i - 16) & 7) == 3) lacc[0] = MFMA16(ones, pf[(i - 16) >> 3][0], lacc[0]); if (((i - 16) & 7) == 5) lacc[1] = MFMA16(ones, pf[(i - 16) >> 3][1], lacc[1]); } } while (0)
#define KPIECE(s_, j) do { if ((j) == 0) { ktile = ((s_) + 2 == swk) ? 0 : ktile + 1; kvo = vlin + (unsigned)min(ktile, 31) * SLOT; SETM0(lds0 + (((s_) & 1) ? kslot1 : kslot0) + wq * 4096); DMAI(kvo, Kh, 0); } \
    if ((j) == 1) DMAI(kvo, Kh, 1024); if ((j) == 2) DMAI(kvo, Kh, 2048); if ((j) == 3) DMAI(kvo, Kh, 3072); } while (0)
#define VPIECE(s_, j) do { if ((j) == 0) { SETM0(lds0 + (((s_) & 1) ? vslot1 : vslot0) + wq * 4096); DMAI(vvo, Vh, 0); } \
    if ((j) == 1) DMAI(vvo, Vh, 1024); if ((j) == 2) DMAI(vvo, Vh, 2048); if ((j) == 3) DMAI(vvo, Vh, 3072); } while (0)
#define MPRE(s_, I0) do { \
    LAS const char* kp = lds + (((s_) & 1) ? kslot1 : kslot0) + klane; \
    LAS const char* vp = lds + (((s_) & 1) ? vslot0 : vslot1) + vlane; \
    _Pragma("unroll") for (int i = I0; i < I0 + PD; ++i) fr_[i] = FRD(i); \
    (void)kp; (void)vp; } while (0)
#define MSLOT(s_, I0, I1, DM) do { \
    LAS const char* kp = lds + (((s_) & 1) ? kslot1 : kslot0) + klane; \
    LAS const char* vp = lds + (((s_) & 1) ? vslot0 : vslot1) + vlane; \
    if (DM) { vtile = ((s_) == swk) ? 0 : vtile + 1; vvo = vlin + (unsigned)min(vtile, 31) * SLOT; } \
    _Pragma("unroll") for (int i = I0; i < I1; ++i) { \
      if (i + PD < I1) fr_[i + PD] = FRD(i + PD); \
      MBODY(i); \
      if ((DM) && (i & 3) == 2 && i < 16) VPIECE(s_, i >> 2); \
      SCHED; \
    } (void)kp; (void)vp; } while (0)
#define SCV(i) sc[((i) >> 2) & 3][(i) >> 4][(i) & 3]
  auto sslot = [&](const int s, const bool SLOW) __attribute__((always_inline)) {
    KPIECE(s, 0);
    bool first = false;
    if (__builtin_expect(SLOW, 0)) {
      const bool swst = (s == swk);
      if (swst) {
        store_ctx(x, 1.0f / lacc[0][0], 1.0f / lacc[1][0]);
#pragma unroll
        for (int i = 0; i < 8; ++i) { o[i][0] = (f32x4){0.f, 0.f, 0.f, 0.f}; o[i][1] = (f32x4){0.f, 0.f, 0.f, 0.f}; }
        lacc[0] = (f32x4){0.f, 0.f, 0.f, 0.f}; lacc[1] = (f32x4){0.f, 0.f, 0.f, 0.f};
      }
      first = (s == 0) || swst;
      const int jb = grp ? s - 15 : s - (sw - 2);
      if (jb >= 0 && jb <= 1) {
        const int dq = 32 * wq + c - 64 * jb - 4 * g;
#pragma unroll
        for (int i = 0; i < 32; ++i) if (16 * ((i >> 2) & 3) + (i & 3) > dq + 16 * (i >> 4)) SCV(i) = -__builtin_inff();
      }
    }
    float pm0 = max3f(SCV(0), SCV(1), SCV(2)), pm1 = max3f(SCV(16), SCV(17), SCV(18));
#pragma unroll
    for (int i = 3; i < 15; i += 2) { pm0 = max3f(pm0, SCV(i), SCV(i + 1)); pm1 = max3f(pm1, SCV(16 + i), SCV(17 + i)); }
    pm0 = max2f(pm0, SCV(15)); pm1 = max2f(pm1, SCV(31));
    KPIECE(s, 1);
    if (__builtin_expect(first || __any(max2f(pm0, pm1) > THR), 0)) {
      const float r0 = xg_max(pm0), r1 = xg_max(pm1);
      const float dl[2] = {first ? r0 : fmaxf(r0, 0.f), first ? r1 : fmaxf(r1, 0.f)};
#pragma unroll
      for (int qt = 0; qt < 2; ++qt) {
        mref[qt] += dl[qt];
#pragma unroll
        for (int kt = 0; kt < 4; ++kt) sc[kt][qt] -= dl[qt];
        if (!first) {
          const float al = __builtin_amdgcn_exp2f(-dl[qt]);
          lacc[qt] *= al;
#pragma unroll
          for (int d = 0; d < 8; ++d) o[d][qt] *= al;
        }
        negm[qt] = (f32x4){-mref[qt], -mref[qt], -mref[qt], -mref[qt]};
      }
    }
    {
      unsigned pw[16];
      SCV(0) = __builtin_amdgcn_exp2f(SCV(0)); SCV(1) = __builtin_amdgcn_exp2f(SCV(1)); SCHED;
#pragma unroll
      for (int i = 0; i < 32; i += 2) {
        if (i + 2 < 32) SCV(i + 2) = __builtin_amdgcn_exp2f(SCV(i + 2));
        pw[i >> 1] = cvtpk(SCV(i), SCV(i + 1));
        if (i + 3 < 32) SCV(i + 3) = __builtin_amdgcn_exp2f(SCV(i + 3));
        if (i == 8) KPIECE(s, 2);
        if (i == 20) KPIECE(s, 3);
        SCHED;
      }
#pragma unroll
      for (int k4 = 0; k4 < 4; ++k4) pf[k4 & 1][k4 >> 1] = __builtin_bit_cast(bf16x8, (u32x4){pw[4 * k4], pw[4 * k4 + 1], pw[4 * k4 + 2], pw[4 * k4 + 3]});
    }
    if (__builtin_expect(SLOW, 0)) {
      if (s + 1 == swk) {
        LAS const char* st = lds + 4 * SLOT + wq * 8192 + lane * 16;
#pragma unroll
        for (int i = 0; i < 8; ++i) qf[i >> 1][i & 1] = *(LAS const bf16x8*)(st + i * 1024);
        mref[0] = 0.f; mref[1] = 0.f; negm[0] = (f32x4){0.f, 0.f, 0.f, 0.f}; negm[1] = (f32x4){0.f, 0.f, 0.f, 0.f};
      }
    }
    asm volatile("" : "+v"(pf[0][0]), "+v"(pf[0][1]), "+v"(pf[1][0]), "+v"(pf[1][1]), "+v"(negm[0]), "+v"(negm[1]));
  };
  auto warm_k3 = [&]() __attribute__((always_inline)) {
#pragma unroll
    for (int i = 0; i < 3; ++i) {
      int sec = (wq * 3 + i) * 64 + lane; sec = sec < 736 ? sec : 735;
      const int kr = 1472 * ((b & 7) >> 1) + 46 * (b >> 3) + (sec >> 4);
      const char* src = (kr < 4096 ? (const char*)Wo + (size_t)kr * 1024 : (const char*)W2 + (size_t)(kr - 4096) * 1024) + (sec & 15) * 64;
      touch64(src, lds0 + 8 * SLOT + wq * 256);
    }
  };
  const unsigned spec = grp ? 0x18001u : (0x0001u | (7u << (sw - 2)));
  LAS char* pb = lds + (wq == 0 ? 9 : (wq == 1 ? 1 : 2 + wq)) * SLOT + lane * 16;
  LAS char* mlp = lds + 2 * SLOT + wq * 1024 + lane * 16;
  STAMP(1, 3);
  if (grp == 1) LBAR();
  MPRE(0, 0); SCHED;
  MSLOT(0, 0, 16, false);
  WBAR(0);
  for (int s = 0; s < 16; ++s) {
    LSTAMP(5);
    sslot(s, (spec >> s) & 1u);
    MPRE(s + 1, 0);
    LSTAMP(6);
    if (__builtin_expect(s == swk, 0)) VBAR(12); else VBAR(4);
    LSTAMP(7);
    MSLOT(s + 1, 0, 32, true);
    LSTAMP(8);
    WBAR(4);
  }
  STAMP(1, 37);
  sslot(16, true);
  WBAR(0);
  if (grp == 1) warm_k3();
  MPRE(17, 16); SCHED;
  MSLOT(17, 16, 32, false);
  STAMP(1, 40);
  if (grp == 0) {
#pragma unroll
    for (int i = 0; i < 16; ++i) *(LAS f32x4*)(pb + i * 1024) = o[i >> 1][i & 1];
    *(LAS f32x4*)mlp = (f32x4){mref[0], lacc[0][0], mref[1], lacc[1][0]};
    LBAR();
  } else {
    const f32x4 ml = *(LAS const f32x4*)mlp;
    float ga[2], gb[2];
#pragma unroll
    for (int qt = 0; qt < 2; ++qt) {
      const float mn = fmaxf(ml[2 * qt], mref[qt]), fa = __builtin_amdgcn_exp2f(ml[2 * qt] - mn), fb = __builtin_amdgcn_exp2f(mref[qt] - mn);
      const float inv = 1.0f / (lacc[qt][0] * fb + ml[2 * qt + 1] * fa);
      ga[qt] = fa * inv; gb[qt] = fb * inv;
    }
#pragma unroll
    for (int i = 0; i < 16; ++i) { const f32x4 pa = *(LAS const f32x4*)(pb + i * 1024); o[i >> 1][i & 1] = o[i >> 1][i & 1] * gb[i & 1] + pa * ga[i & 1]; }
    store_ctx(15 - x, 1.0f, 1.0f);
  }
#undef FRD
#undef MSLOT
#undef MPRE
#undef MBODY
#undef KPIECE
#undef VPIECE
#undef SCV
  STAMP(1, 41); STAMPR(1, 63);
}

constexpr int K3_NS = 5, K3_LDS = K3_NS * 32768;
__global__ __launch_bounds__(512, 2) void k3_out(const u16* __restrict__ Abuf, const u16* __restrict__ Wo, const u16* __restrict__ W2, const u16* __restrict__ x,
                                                 const u16* __restrict__ b2, unsigned* __restrict__ slabs, u16* __restrict__ out) {
  extern __shared__ __attribute__((aligned(16))) char smem[];
  LAS char* lds = (LAS char*)smem;
  const unsigned lds0 = (unsigned)(uintptr_t)lds;
  const int tid = threadIdx.x, lane = tid & 63, wid = rfl(tid >> 6), kh = wid >> 2, wr = (wid >> 1) & 1, wc = wid & 1, fr = lane & 15, fq = lane >> 4;
  const int b = blockIdx.x, xcd = b & 7, idx = b >> 3, sp = xcd >> 1, rt = (xcd & 1) * 8 + (idx >> 2), ct = idx & 3;
  const int kt0 = sp * 23;
  const char* Ab = (const char*)Abuf + (size_t)(rt * NKT + kt0) * 16384;
  const unsigned voffa = (unsigned)(wid * 2048 + lane * 16);
  const unsigned voffb = (unsigned)((32 * (wid >> 2) + 16 * (lane >> 5) + 4 * (wid & 3) + ((lane >> 3) & 3)) * DM + 128 * ct + 16 * (((lane >> 1) & 3) ^ (((lane >> 4) & 1) | ((wid & 1) << 1))) + 8 * (lane & 1)) * 2u;
  auto stage = [&](int it) __attribute__((always_inline)) {
    const int kt = kt0 + it;
    const char* wb = kt < 64 ? (const char*)Wo + (size_t)kt * 64 * DM * 2 : (const char*)W2 + (size_t)(kt - 64) * 64 * DM * 2;
    const unsigned d = lds0 + (it % K3_NS) * 32768 + wid * 2048;
    dma2k(Ab + (size_t)it * 16384, voffa, d);
    dma1k(wb, voffb, d + 16384); dma1k(wb + 128, voffb, d + 16384 + 1024);
  };
  const int aoff = (4 * kh + fq) * 2048 + (wr * 64 + fr) * 16;
  int boff[4];
#pragma unroll
  for (int n = 0; n < 4; ++n) boff[n] = 16384 + (4 * kh + fq) * 2048 + wc * 1024 + ((lane >> 2) & 3) * 128 + ((n ^ (((lane >> 3) & 1) | ((fq & 1) << 1))) * 32) + 8 * (lane & 3);
  f32x4 acc[4][4];
#pragma unroll
  for (int m = 0; m < 4; ++m)
#pragma unroll
    for (int n = 0; n < 4; ++n) acc[m][n] = (f32x4){0.f, 0.f, 0.f, 0.f};
  bf16x8 a0[4], b0[4], a1[4], b1[4];
#define K3_RDB(bf, it_, n) bf[n] = vtr2(lds + ((it_) % K3_NS) * 32768 + boff[n], lds + ((it_) % K3_NS) * 32768 + boff[n] + 512)
#define K3_RDA(af, it_, m) af[m] = *(LAS const bf16x8*)(lds + ((it_) % K3_NS) * 32768 + aoff + (m) * 256)
#define K3_STEP(it_, ac, bc, an, bn, RD, ST) do { \
    if ((ST) && kh == 0) stage((it_) + 5); \
    _Pragma("unroll") for (int m = 0; m < 4; ++m) { \
      if (RD) { K3_RDB(bn, (it_) + 1, m); K3_RDA(an, (it_) + 1, m); } \
      _Pragma("unroll") for (int n = 0; n < 4; ++n) acc[m][n] = MFMA16(bc[n], ac[m], acc[m][n]); \
      SCHED; \
      if (m == 1 && (ST) && kh == 1) { stage((it_) + 5); SCHED; } } } while (0)
  STAMPR(2, 62); STAMP(2, 0);
  stage(0); stage(1); stage(2); stage(3); stage(4);
  WBAR(16);
#pragma unroll
  for (int n = 0; n < 4; ++n) { K3_RDB(b0, 0, n); K3_RDA(a0, 0, n); }
  STAMP(2, 1);
#pragma unroll
  for (int j = 0; j < 18; j += 2) {
    WBAR(12); K3_STEP(j, a0, b0, a1, b1, true, true);
    WBAR(12); K3_STEP(j + 1, a1, b1, a0, b0, true, true);
  }
  WBAR(12); K3_STEP(18, a0, b0, a1, b1, true, false);
  WBAR(8); K3_STEP(19, a1, b1, a0, b0, true, false);
  WBAR(4); K3_STEP(20, a0, b0, a1, b1, true, false);
  WBAR(0); K3_STEP(21, a1, b1, a0, b0, true, false);
  K3_STEP(22, a0, b0, a1, b1, false, false);
#undef K3_RDA
#undef K3_RDB
#undef K3_STEP
  f32x4 r2[2][4];
  {
    LAS char* mine = lds + wid * 8192 + lane * 16; LAS const char* theirs = lds + (wid ^ 4) * 8192 + lane * 16;
#pragma unroll
    for (int mm = 0; mm < 2; ++mm)
#pragma unroll
      for (int n = 0; n < 4; ++n) *(LAS f32x4*)(mine + (mm * 4 + n) * 1024) = kh ? acc[mm][n] : acc[2 + mm][n];
    LBAR();
#pragma unroll
    for (int mm = 0; mm < 2; ++mm)
#pragma unroll
      for (int n = 0; n < 4; ++n) { const f32x4 t = *(LAS const f32x4*)(theirs + (mm * 4 + n) * 1024); r2[mm][n] = kh ? t + acc[2 + mm][n] : acc[mm][n] + t; }
  }
  STAMP(2, 30);
  const int tile = rt * 4 + ct;
  const __amdgpu_buffer_rsrc_t rs = __builtin_amdgcn_make_buffer_rsrc((void*)(slabs + (size_t)tile * 12 * 2048), (short)0, 12 * 8192, 0x00020000);
  const int sl = wid * 1024 + lane * 16;
  f32x4 own[2] = {r2[0][0], r2[1][0]};
#pragma unroll
  for (int q = 0; q < 4; ++q) {
    if (q != sp) {
      u32x4 w = __builtin_bit_cast(u32x4, pack8(r2[0][q], r2[1][q], 1.0f));
#pragma unroll
      for (int e = 0; e < 4; ++e) w[e] = w[e] == SENT ? 0x7fc07fc0u : w[e];
      __builtin_amdgcn_raw_buffer_store_b128(w, rs, (q * 3 + (sp > q ? sp - 1 : sp)) * 8192 + sl, 0, 17);
    } else { own[0] = r2[0][q]; own[1] = r2[1][q]; }
  }
  STAMP(2, 31); STAMPR(2, 63);
  {
    const int col = ct * 128 + 64 * wc + 16 * sp + 4 * fq;
    const size_t roff = (size_t)(rt * 128 + 64 * wr + 32 * kh + fr) * DM + col;
    const u32x2 bb = *(const u32x2*)(b2 + col);
    u32x2 xx[2];
#pragma unroll
    for (int mm = 0; mm < 2; ++mm) xx[mm] = *(const u32x2*)(x + roff + (size_t)mm * 16 * DM);
    u32x4 t[3];
    for (int tries = 0; tries < (1 << 14); ++tries) {
      bool ok = true;
      asm volatile("" ::: "memory");
#pragma unroll
      for (int o = 0; o < 3; ++o) t[o] = __builtin_amdgcn_raw_buffer_load_b128(rs, (sp * 3 + o) * 8192 + sl, 0, 17);
#pragma unroll
      for (int o = 0; o < 3; ++o)
#pragma unroll
        for (int e = 0; e < 4; ++e) ok = ok && (t[o][e] != SENT);
      if (__all(ok)) break;
      __builtin_amdgcn_s_sleep(4);
    }
#pragma unroll
    for (int mm = 0; mm < 2; ++mm) {
      f32x4 tv[3];
#pragma unroll
      for (int o = 0; o < 3; ++o) tv[o] = (f32x4){bflo(t[o][2 * mm]), bfhi(t[o][2 * mm]), bflo(t[o][2 * mm + 1]), bfhi(t[o][2 * mm + 1])};
      const f32x4 p0 = sp == 0 ? own[mm] : tv[0], p1 = sp == 1 ? own[mm] : (sp < 1 ? tv[0] : tv[1]), p2 = sp == 2 ? own[mm] : (sp < 2 ? tv[1] : tv[2]), p3 = sp == 3 ? own[mm] : tv[2];
      const f32x4 a = ((p0 + p1) + p2) + p3;
      u32x2 w = {cvtpk(a[0] + bflo(xx[mm][0]) + bflo(bb[0]), a[1] + bfhi(xx[mm][0]) + bfhi(bb[0])), cvtpk(a[2] + bflo(xx[mm][1]) + bflo(bb[1]), a[3] + bfhi(xx[mm][1]) + bfhi(bb[1]))};
      *(u32x2*)(out + roff + (size_t)mm * 16 * DM) = w;
    }
  }
  STAMP(2, 32); STAMPR(2, 61);
}

extern "C" void kernel_launch(void* const* d_in, const int* in_sizes, int n_in, void* d_out, int out_size, void* d_ws, size_t ws_size,
                              hipStream_t stream) {
  const u16* x = (const u16*)d_in[0];
  const u16* ckv = (const u16*)d_in[1];
  const u16* wq = (const u16*)d_in[2];
  const u16* wuk = (const u16*)d_in[3];
  const u16* wuv = (const u16*)d_in[4];
  const u16* wo = (const u16*)d_in[5];
  const u16* w1 = (const u16*)d_in[7];
  const u16* b1 = (const u16*)d_in[8];
  const u16* w2 = (const u16*)d_in[9];
  const u16* b2 = (const u16*)d_in[10];
  char* ws = (char*)d_ws;
  u16* Kws = (u16*)(ws + OFF_K); u16* Vws = (u16*)(ws + OFF_V); u16* xn_t = (u16*)(ws + OFF_XN); u16* Abuf = (u16*)(ws + OFF_A);
  unsigned* slabs = (unsigned*)(ws + OFF_SLAB);
  (void)hipFuncSetAttribute((const void*)k1_kv, hipFuncAttributeMaxDynamicSharedMemorySize, K1_LDS + 256);
  (void)hipFuncSetAttribute((const void*)k2_attn, hipFuncAttributeMaxDynamicSharedMemorySize, K2_LDS);
  (void)hipFuncSetAttribute((const void*)k3_out, hipFuncAttributeMaxDynamicSharedMemorySize, K3_LDS);
  k1_kv<<<256, 512, K1_LDS + 256, stream>>>(x, ckv, wuk, wuv, Kws, Vws, xn_t, wq, w1, wo, w2);
  k2_attn<<<256, 512, K2_LDS, stream>>>(xn_t, wq, w1, b1, Kws, Vws, Abuf, slabs, wo, w2);
  k3_out<<<256, 512, K3_LDS, stream>>>(Abuf, wo, w2, x, b2, slabs, (u16*)d_out);

}
```

```cpp
#include <hip/hip_runtime.h>
#include <hip/hip_bf16.h>
#include <stdint.h>
#include <type_traits>

typedef unsigned short u16;
typedef short bf16x8 __attribute__((ext_vector_type(8)));
typedef short s16x4 __attribute__((ext_vector_type(4)));
typedef short v4i16_t __attribute__((ext_vector_type(4)));
typedef float f32x2 __attribute__((ext_vector_type(2)));
typedef float f32x4 __attribute__((ext_vector_type(4)));
typedef unsigned u32x4 __attribute__((ext_vector_type(4)));
typedef unsigned u32x2 __attribute__((ext_vector_type(2)));
typedef __bf16 bf16x2_t __attribute__((ext_vector_type(2)));
#define LAS __attribute__((address_space(3)))
#define DI __device__ __forceinline__

constexpr int SEQ = 2048, DM = 512, NH = 32, HD = 128, FF = 1792, KTOT = NH * HD + FF, NKT = KTOT / 64;
constexpr size_t MiB = 1024 * 1024;
constexpr unsigned SENT = 0xffffffffu;
constexpr size_t OFF_K = 0, OFF_V = 16 * MiB, OFF_XN = 32 * MiB, OFF_A = 34 * MiB, OFF_SLAB = 58 * MiB;

DI unsigned cvtpk(float lo, float hi) { f32x2 v = {lo, hi}; bf16x2_t b = __builtin_convertvector(v, bf16x2_t); return __builtin_bit_cast(unsigned, b); }
DI float rbf(float x) { return __uint_as_float(cvtpk(x, x) & 0xffff0000u); }
DI float bflo(unsigned w) { return __uint_as_float(w << 16); }
DI float bfhi(unsigned w) { return __uint_as_float(w & 0xffff0000u); }
DI s16x4 vtr(LAS const char* p) { return __builtin_bit_cast(s16x4, __builtin_amdgcn_ds_read_tr16_b64_v4i16((LAS v4i16_t*)p)); }
DI bf16x8 vtr2(LAS const char* p0, LAS const char* p1) { s16x4 a = vtr(p0), b = vtr(p1); return __builtin_shufflevector(a, b, 0, 1, 2, 3, 4, 5, 6, 7); }
DI float max3f(float a, float b, float c) { float r; asm("v_max3_f32 %0, %1, %2, %3" : "=v"(r) : "v"(a), "v"(b), "v"(c)); return r; }
DI float max2f(float a, float b) { float r; asm("v_max_f32_e32 %0, %1, %2" : "=v"(r) : "v"(a), "v"(b)); return r; }
DI int rfl(int x) { return __builtin_amdgcn_readfirstlane(x); }
#define SCHED __builtin_amdgcn_sched_barrier(0)
#define WAIT_V(n) asm volatile("s_waitcnt vmcnt(" #n ")" ::: "memory")
#define WAIT_L(n) asm volatile("s_waitcnt lgkmcnt(" #n ")" ::: "memory")
#define BAR __builtin_amdgcn_s_barrier()
#define MFMA16(a, b, c) __builtin_amdgcn_mfma_f32_16x16x32_bf16((a), (b), (c), 0, 0, 0)
#define STAMP(kid, i) do {} while (0)
#define STAMPR(kid, i) do {} while (0)

DI void dma1k(const void* sbase, unsigned voff, unsigned ldsdst) {
  unsigned sv;
  asm volatile("s_mov_b32 %0, m0\n\ts_mov_b32 m0, %3\n\ts_nop 0\n\t"
               "global_load_lds_dwordx4 %1, %2\n\t"
               "s_mov_b32 m0, %0" : "=&s"(sv) : "v"(voff), "s"(sbase), "s"(ldsdst) : "memory");
}
DI void touch64(const void* p, unsigned ldsdump) {
  unsigned sv;
  asm volatile("s_mov_b32 %0, m0\n\ts_mov_b32 m0, %2\n\ts_nop 0\n\t"
               "global_load_lds_dword %1, off\n\t"
               "s_mov_b32 m0, %0" : "=&s"(sv) : "v"(p), "s"(ldsdump) : "memory");
}
DI void dma2k(const void* sbase, unsigned voff, unsigned ldsdst) {
  unsigned sv;
  asm volatile("s_mov_b32 %0, m0\n\ts_mov_b32 m0, %3\n\ts_nop 0\n\t"
               "global_load_lds_dwordx4 %1, %2\n\t"
               "global_load_lds_dwordx4 %1, %2 offset:1024\n\t"
               "s_mov_b32 m0, %0" : "=&s"(sv) : "v"(voff), "s"(sbase), "s"(ldsdst) : "memory");
}
DI void dma4k(const void* sbase, unsigned voff, unsigned ldsdst) {
  unsigned sv;
  asm volatile("s_mov_b32 %0, m0\n\ts_mov_b32 m0, %3\n\ts_nop 0\n\t"
               "global_load_lds_dwordx4 %1, %2\n\t"
               "global_load_lds_dwordx4 %1, %2 offset:1024\n\t"
               "global_load_lds_dwordx4 %1, %2 offset:2048\n\t"
               "global_load_lds_dwordx4 %1, %2 offset:3072\n\t"
               "s_mov_b32 m0, %0" : "=&s"(sv) : "v"(voff), "s"(sbase), "s"(ldsdst) : "memory");
}

constexpr int K1_HTB = 128 * 64 * 2, K1_LDS = 8 * K1_HTB;
DI int lds_byte(int r, int c) { const int st = (r >> 4) * 2 + (c >> 5), rr = r & 15, cc = c & 31, ob = rr * 64 + cc * 2; return st * 1024 + (ob ^ (((ob >> 9) & 1) << 5)); }
DI void stage_rc(int b, int& R, int& C) { const int st = b / 1024, sb = b % 1024, swz = sb ^ (((sb >> 9) & 1) << 5); R = (st >> 1) * 16 + swz / 64; C = (st & 1) * 32 + (swz % 64) / 2; }

template <bool ISV>
DI void k1_body(const u16* __restrict__ A, const u16* __restrict__ W, u16* __restrict__ out, int pm, int pn, LAS char* lds,
                const u16* __restrict__ xrow, u16* __restrict__ xn_t, int row, const char* __restrict__ warm0, const char* __restrict__ warm1) {
  const int tid = threadIdx.x, wid = rfl(tid >> 6), lane = tid & 63, wr = wid >> 2, wc = wid & 3, fr = lane & 15, fq = lane >> 4;
  STAMPR(0, 62); STAMP(0, 0);
  u32x4 xv;
  asm volatile("global_load_dwordx4 %0, %1, off" : "=v"(xv) : "v"(xrow + lane * 8) : "memory");
  unsigned voffA[2], voffB[2];
#pragma unroll
  for (int i = 0; i < 2; ++i) {
    int R, C; stage_rc(tid * 16 + i * 8192, R, C); voffA[i] = (unsigned)(R * DM + C) * 2u;
    const int b = tid * 16 + i * 8192, kb = b >> 11, nh = (b >> 10) & 1, kr = (b >> 7) & 7, c16 = (b >> 4) & 7;
    const int slot = (c16 >> 1) ^ (((kr >> 1) & 1) | ((kb & 1) << 1));
    voffB[i] = (unsigned)((8 * kb + kr) * 4096 + 64 * nh + 16 * slot + 8 * (c16 & 1)) * 2u;
  }
  const unsigned ldsw = (unsigned)wid * 1024u;
  const int aoff = lds_byte(wr * 64 + fr, fq * 8);
  const int boff0 = fq * 2048 + (wc >> 1) * 1024 + ((lane >> 2) & 3) * 128 + (((2 * (wc & 1)) ^ (((lane >> 3) & 1) | ((fq & 1) << 1))) * 32) + 8 * (lane & 3), boff1 = boff0 ^ 32;
  const char* cA = (const char*)A + (size_t)pm * 256 * DM * 2;
  const char* cB = (const char*)W + (size_t)pn * 256 * 2;
  constexpr size_t AK = 64 * 2, AH = (size_t)128 * DM * 2, BK_ = (size_t)64 * 4096 * 2, BH = 128 * 2;
#define SA(b, h) (((b) * 2 + (h)) * K1_HTB)
#define SB(b, h) ((4 + (b) * 2 + (h)) * K1_HTB)
#define STAGE(bufoff, gbase, voff) do { _Pragma("unroll") for (int _i = 0; _i < 2; ++_i) \
    __builtin_amdgcn_global_load_lds((const unsigned*)((const char*)(gbase) + (voff)[_i]), (LAS unsigned*)(lds + (bufoff) + ldsw + _i * 8192), 16, 0, 0); } while (0)
#define STA(b, h, kt) STAGE(SA(b, h), cA + (h) * AH + (kt) * AK, voffA)
#define STB(b, h, kt) STAGE(SB(b, h), cB + (h) * BH + (kt) * BK_, voffB)
#define LDA(dst, b, h) do { _Pragma("unroll") for (int m = 0; m < 4; ++m) _Pragma("unroll") for (int k = 0; k < 2; ++k) dst[m][k] = *(const LAS bf16x8*)(lds + SA(b, h) + aoff + m * 2048 + k * 1024); } while (0)
#define LDB(dst, b, h) do { _Pragma("unroll") for (int n = 0; n < 2; ++n) _Pragma("unroll") for (int k = 0; k < 2; ++k) \
    dst[n][k] = vtr2(lds + SB(b, h) + (n ? boff1 : boff0) + k * 8192, lds + SB(b, h) + (n ? boff1 : boff0) + k * 8192 + 512); } while (0)
#define MMA(ai, bj, At, Bt) do { __builtin_amdgcn_s_setprio(1); _Pragma("unroll") for (int m = 0; m < 4; ++m) _Pragma("unroll") for (int n = 0; n < 2; ++n) _Pragma("unroll") for (int k = 0; k < 2; ++k) \
    acc[ai][bj][m][n] = ISV ? MFMA16(At[m][k], Bt[n][k], acc[ai][bj][m][n]) : MFMA16(Bt[n][k], At[m][k], acc[ai][bj][m][n]); __builtin_amdgcn_s_setprio(0); } while (0)
  f32x4 acc[2][2][4][2];
#pragma unroll
  for (int a = 0; a < 2; ++a)
#pragma unroll
    for (int b = 0; b < 2; ++b)
#pragma unroll
      for (int m = 0; m < 4; ++m)
#pragma unroll
        for (int n = 0; n < 2; ++n) acc[a][b][m][n] = (f32x4){0.f, 0.f, 0.f, 0.f};
  bf16x8 At[4][2], B0[2][2], B1[2][2];
  constexpr int nt = DM / 64;
  STB(0, 0, 0); STA(0, 0, 0); STB(0, 1, 0); STA(0, 1, 0);
  touch64(warm0, (unsigned)(uintptr_t)lds + K1_LDS); touch64(warm1, (unsigned)(uintptr_t)lds + K1_LDS);
  if (wr == 1) BAR;
  asm volatile("s_waitcnt vmcnt(6)" : "+v"(xv) :: "memory");
  BAR;
  {
    float v[8];
#pragma unroll
    for (int i = 0; i < 4; ++i) { v[2 * i] = bflo(xv[i]); v[2 * i + 1] = bfhi(xv[i]); }
    float s = 0.f;
#pragma unroll
    for (int i = 0; i < 8; ++i) s += v[i];
#pragma unroll
    for (int o = 32; o >= 1; o >>= 1) s += __shfl_xor(s, o);
    const float mean = s * (1.0f / DM);
    float q = 0.f;
#pragma unroll
    for (int i = 0; i < 8; ++i) { const float d = v[i] - mean; q += d * d; }
#pragma unroll
    for (int o = 32; o >= 1; o >>= 1) q += __shfl_xor(q, o);
    const float mb = rbf(mean), vb = rbf(q * (1.0f / DM));
    const float den = rbf(sqrtf(rbf(vb + 1e-5f)));
    u32x4 o4;
#pragma unroll
    for (int i = 0; i < 4; ++i) o4[i] = cvtpk(rbf(v[2 * i] - mb) / den, rbf(v[2 * i + 1] - mb) / den);
    *(u32x4*)(xn_t + ((size_t)((row >> 4) * 16 + (lane >> 2)) * 64 + (lane & 3) * 16 + (row & 15)) * 8) = o4;
  }
  STB(1, 0, 1); STA(1, 0, 1); STB(1, 1, 1);
  WAIT_V(6); BAR;
  STAMP(0, 1);
  for (int t = 0; t < nt - 2; t += 2) {
    LDB(B0, 0, 0); SCHED; LDA(At, 0, 0); STA(1, 1, t + 1);
    WAIT_L(8); BAR; WAIT_L(0); MMA(0, 0, At, B0); BAR; SCHED;
    LDB(B1, 0, 1); STB(0, 0, t + 2);
    BAR; WAIT_L(0); MMA(0, 1, At, B1); BAR;
    LDA(At, 0, 1); STA(0, 0, t + 2);
    BAR; WAIT_L(0); MMA(1, 0, At, B0); BAR; SCHED;
    STB(0, 1, t + 2);
    WAIT_V(6); BAR; MMA(1, 1, At, B1); BAR;
    LDB(B0, 1, 0); SCHED; LDA(At, 1, 0); STA(0, 1, t + 2);
    WAIT_L(8); BAR; WAIT_L(0); MMA(0, 0, At, B0); BAR; SCHED;
    LDB(B1, 1, 1); STB(1, 0, t + 3);
    BAR; WAIT_L(0); MMA(0, 1, At, B1); BAR;
    LDA(At, 1, 1); STA(1, 0, t + 3);
    BAR; WAIT_L(0); MMA(1, 0, At, B0); BAR; SCHED;
    STB(1, 1, t + 3);
    WAIT_V(6); BAR; MMA(1, 1, At, B1); BAR;
  }
  { LDB(B0, 0, 0); WAIT_V(0); LDA(At, 0, 0); STA(1, 1, nt - 1);
    BAR; WAIT_L(0); MMA(0, 0, At, B0); BAR;
    LDB(B1, 0, 1); BAR; WAIT_L(0); MMA(0, 1, At, B1); BAR;
    LDA(At, 0, 1); WAIT_V(4); BAR; WAIT_L(0); MMA(1, 0, At, B0); MMA(1, 1, At, B1); BAR; }
  auto epi = [&](const int ai, const int bj) __attribute__((always_inline)) {
    const int head = 2 * pn + bj, tile = 4 * pm + 2 * ai + wr;
    const __amdgpu_buffer_rsrc_t trs = __builtin_amdgcn_make_buffer_rsrc((void*)(out + (size_t)(head * 32 + tile) * 8192), (short)0, 16384, 0x00020000);
    if (!ISV) {
#pragma unroll
      for (int m = 0; m < 4; ++m) {
        const f32x4 a0 = acc[ai][bj][m][0], a1 = acc[ai][bj][m][1];
        u32x4 w = {cvtpk(a0[0], a0[1]), cvtpk(a0[2], a0[3]), cvtpk(a1[0], a1[1]), cvtpk(a1[2], a1[3])};
        __builtin_amdgcn_raw_buffer_store_b128(w, trs, ((4 * wc + fq) * 64 + 16 * m + fr) * 16, 0, 0);
      }
    } else {
#pragma unroll
      for (int mp = 0; mp < 2; ++mp)
#pragma unroll
        for (int n = 0; n < 2; ++n) {
          const f32x4 a0 = acc[ai][bj][2 * mp][n], a1 = acc[ai][bj][2 * mp + 1][n];
          u32x4 w = {cvtpk(a0[0], a0[1]), cvtpk(a0[2], a0[3]), cvtpk(a1[0], a1[1]), cvtpk(a1[2], a1[3])};
          __builtin_amdgcn_raw_buffer_store_b128(w, trs, ((4 * mp + fq) * 128 + 32 * wc + 16 * n + fr) * 16, 0, 0);
        }
    }
  };
  { LDB(B0, 1, 0); LDA(At, 1, 0); WAIT_V(2); BAR; WAIT_L(0); MMA(0, 0, At, B0); BAR;
    LDB(B1, 1, 1); WAIT_V(0); BAR; WAIT_L(0); MMA(0, 1, At, B1); BAR;
    LDA(At, 1, 1); SCHED; epi(0, 0); SCHED; BAR; WAIT_L(0); MMA(1, 0, At, B0); MMA(1, 1, At, B1); BAR; }
  STAMP(0, 2);
  epi(0, 1); epi(1, 0); epi(1, 1);
  if (wr == 0) BAR;
  STAMP(0, 3); STAMPR(0, 63);
#undef SA
#undef SB
#undef STAGE
#undef STA
#undef STB
#undef LDA
#undef LDB
#undef MMA
}

__global__ __launch_bounds__(512, 2) void k1_kv(const u16* __restrict__ x, const u16* __restrict__ ckv, const u16* __restrict__ wuk, const u16* __restrict__ wuv,
                                                u16* __restrict__ Kws, u16* __restrict__ Vws, u16* __restrict__ xn_t,
                                                const u16* __restrict__ wq, const u16* __restrict__ w1, const u16* __restrict__ wo, const u16* __restrict__ w2) {
  extern __shared__ __attribute__((aligned(16))) char smem[];
  const int b = blockIdx.x, xcd = b & 7, idx = b >> 3;
  const int pnn = ((idx >> 4) << 4) + xcd * 2 + ((idx >> 3) & 1), pm = idx & 7;
  const int row = b * 8 + rfl(threadIdx.x >> 6);
  const int gid = b * 512 + threadIdx.x, l0 = gid < 94208 ? gid >> 1 : 47103, h0 = (gid & 1) * 64;
  const char* warm0 = (l0 < 32768 ? (const char*)wq + (size_t)l0 * 128 : (const char*)w1 + (size_t)(l0 - 32768) * 128) + h0;
  const char* warm1 = (l0 < 32768 ? (const char*)wo + (size_t)l0 * 128 : (const char*)w2 + (size_t)(l0 - 32768) * 128) + h0;
  if (pnn < 16) k1_body<false>(ckv, wuk, Kws, pm, pnn, (LAS char*)smem, x + (size_t)row * DM, xn_t, row, warm0, warm1);
  else          k1_body<true>(ckv, wuv, Vws, pm, pnn - 16, (LAS char*)smem, x + (size_t)row * DM, xn_t, row, warm0, warm1);
}

#define LSTAMP(k) do {} while (0)
constexpr int SLOT = 16384, K2_LDS = 10 * SLOT;
constexpr float QSCALE = 0.08838834764831845f * 1.4426950408889634f;
constexpr float THR = 32.f;
#define WBAR(N) do { asm volatile("s_waitcnt vmcnt(" #N ") lgkmcnt(0)\n\ts_barrier" ::: "memory"); SCHED; } while (0)
#define LBAR() do { asm volatile("s_waitcnt lgkmcnt(0)\n\ts_barrier" ::: "memory"); SCHED; } while (0)
#define VBAR(N) do { asm volatile("s_waitcnt vmcnt(" #N ")\n\ts_barrier" ::: "memory"); SCHED; } while (0)
DI float xg_max(float v) { v = fmaxf(v, __shfl_xor(v, 16)); return fmaxf(v, __shfl_xor(v, 32)); }
DI bf16x8 pack8(const f32x4& a, const f32x4& b, float sc) {
  return __builtin_bit_cast(bf16x8, (u32x4){cvtpk(a[0] * sc, a[1] * sc), cvtpk(a[2] * sc, a[3] * sc), cvtpk(b[0] * sc, b[1] * sc), cvtpk(b[2] * sc, b[3] * sc)});
}

__global__ __launch_bounds__(512, 2) void k2_attn(const u16* __restrict__ xn_t, const u16* __restrict__ Wq, const u16* __restrict__ W1, const u16* __restrict__ b1,
                                                  const u16* __restrict__ Kws, const u16* __restrict__ Vws, u16* __restrict__ Abuf, unsigned* __restrict__ slabs,
                                                  const u16* __restrict__ Wo, const u16* __restrict__ W2) {
  extern __shared__ __attribute__((aligned(16))) char smem[];
  LAS char* lds = (LAS char*)smem;
  const unsigned lds0 = (unsigned)(uintptr_t)lds;
  const int tid = threadIdx.x, lane = tid & 63, wid = rfl(tid >> 6), grp = wid >> 2, wq = wid & 3, c = lane & 15, g = lane >> 4;
  const int b = blockIdx.x, head = (b & 7) * 4 + (b >> 6), x = (b >> 3) & 7;
  const bool doF = head < 28;
  const int fh = doF ? head : 27;
  const int sw = 2 * x + 2;
  const int blk0 = grp ? 15 - x : x;
  const char* Kh = (const char*)Kws + (size_t)head * 32 * SLOT;
  const char* Vh = (const char*)Vws + (size_t)head * 32 * SLOT;
  const unsigned vlin = (unsigned)(wq * 4096 + lane * 16);
  const int tb0 = grp ? 15 - 2 * x : 0;
  const __amdgpu_buffer_rsrc_t arsrc = __builtin_amdgcn_make_buffer_rsrc((void*)Abuf, (short)0, 16 * NKT * 16384, 0x00020000);
  const int cvoff = (32 * wq + c) * 16 + g * 2048;

  bf16x8 qf[4][2];
  STAMPR(1, 62); STAMP(1, 0);
  auto prologue = [&](auto ftag) __attribute__((always_inline)) {
    constexpr bool DOF = decltype(ftag)::value;
    const char* xbase = (const char*)xn_t + (size_t)(blk0 * 8 + 2 * wq) * 16384;
    const unsigned xvo = lane * 16;
    const int dsl = 16 * (((lane >> 1) & 3) ^ (((lane >> 4) & 1) | ((wid & 1) << 1))) + 8 * (lane & 1);
    const unsigned voffq = (unsigned)((8 * wid + (lane >> 3)) * 4096 + head * 128 + dsl) * 2u;
    const unsigned voff1 = (unsigned)((8 * wid + (lane >> 3)) * FF + fh * 64 + dsl) * 2u;
    constexpr int CH = 24576;
    auto stage_w = [&](int ch) __attribute__((always_inline)) {
      const char* q0 = (const char*)Wq + (size_t)ch * 64 * 4096 * 2;
      const char* f0 = (const char*)W1 + (size_t)ch * 64 * FF * 2;
      const unsigned d0 = lds0 + (ch & 3) * CH + wid * 3072;
      dma1k(q0, voffq, d0); dma1k(q0 + 128, voffq, d0 + 1024); dma1k(f0, voff1, d0 + 2048);
    };
    int wb[4];
#pragma unroll
    for (int sl = 0; sl < 4; ++sl) wb[sl] = g * 3072 + ((lane >> 2) & 3) * 128 + ((sl ^ (((lane >> 3) & 1) | ((g & 1) << 1))) * 32) + 8 * (lane & 3);
#define WFRAG(ch, ks, t) vtr2(lds + ((ch) & 3) * CH + wb[(t) & 3] + (ks) * 12288 + ((t) >> 2) * 1024, lds + ((ch) & 3) * CH + wb[(t) & 3] + (ks) * 12288 + ((t) >> 2) * 1024 + 512)
    f32x4 aq[12][2];
#pragma unroll
    for (int t = 0; t < 12; ++t) { aq[t][0] = (f32x4){0.f, 0.f, 0.f, 0.f}; aq[t][1] = (f32x4){0.f, 0.f, 0.f, 0.f}; }
    bf16x8 xr[4][4];
#define XLOAD(ch) asm volatile("global_load_dwordx4 %0, %4, %5\n\tglobal_load_dwordx4 %1, %4, %5 offset:1024\n\tglobal_load_dwordx4 %2, %4, %6\n\tglobal_load_dwordx4 %3, %4, %6 offset:1024" \
      : "=&v"(xr[(ch) & 3][0]), "=&v"(xr[(ch) & 3][1]), "=&v"(xr[(ch) & 3][2]), "=&v"(xr[(ch) & 3][3]) : "v"(xvo), "s"(xbase + (ch) * 2048), "s"(xbase + 16384 + (ch) * 2048) : "memory")
#define CWAIT(ch, N) do { asm volatile("s_waitcnt vmcnt(" #N ") lgkmcnt(0)\n\ts_barrier" : "+v"(xr[(ch) & 3][0]), "+v"(xr[(ch) & 3][1]), "+v"(xr[(ch) & 3][2]), "+v"(xr[(ch) & 3][3]) :: "memory"); SCHED; } while (0)
    constexpr int WPD = 6;
    bf16x8 wa[24 + WPD];
    auto compute = [&](int ch, auto&& act) __attribute__((always_inline)) {
#pragma unroll
      for (int i = 0; i < 24; ++i) {
        const int ks = i / 12, t = i % 12, in = i + WPD, chn = in < 24 ? ch : ch + 1, ksn = (in % 24) / 12, tn = in % 12;
        if ((in < 24 || ch < 7) && (tn < 8 || DOF)) wa[in] = WFRAG(chn, ksn, tn);
        if (t < 8 || DOF) { aq[t][0] = MFMA16(wa[i], xr[ch & 3][ks], aq[t][0]); aq[t][1] = MFMA16(wa[i], xr[ch & 3][2 + ks], aq[t][1]); }
        if (i % 6 == 1) act(i / 6);
        SCHED;
      }
#pragma unroll
      for (int j = 0; j < WPD; ++j) wa[j] = wa[24 + j];
    };
    u32x2 bb[4];
    const u16* b1p = b1 + fh * 64; const int bvo = g * 8;
    XLOAD(0); stage_w(0); XLOAD(1); stage_w(1); XLOAD(2); stage_w(2);
    CWAIT(0, 14); STAMP(1, 1);
#pragma unroll
    for (int j = 0; j < WPD; ++j) wa[j] = WFRAG(0, 0, j);
    CWAIT(1, 7); compute(0, [&](int i) __attribute__((always_inline)) { if (i == 0) XLOAD(3); if (i == 1) stage_w(3); });
    CWAIT(2, 7); compute(1, [&](int i) __attribute__((always_inline)) { if (i == 0) XLOAD(4); if (i == 1) stage_w(4); });
    CWAIT(3, 7); compute(2, [&](int i) __attribute__((always_inline)) { if (i == 0) XLOAD(5); if (i == 1) stage_w(5); });
    CWAIT(4, 7); compute(3, [&](int i) __attribute__((always_inline)) { if (i == 0) XLOAD(6); if (i == 1) stage_w(6); });
    CWAIT(5, 7); compute(4, [&](int i) __attribute__((always_inline)) { if (i == 0) XLOAD(7); if (i == 1) stage_w(7); });
    CWAIT(6, 7);
    compute(5, [&](int i) __attribute__((always_inline)) {
      if (i == 0) asm volatile("global_load_dwordx2 %0, %4, %5\n\tglobal_load_dwordx2 %1, %4, %5 offset:32\n\tglobal_load_dwordx2 %2, %4, %5 offset:64\n\tglobal_load_dwordx2 %3, %4, %5 offset:96"
                               : "=&v"(bb[0]), "=&v"(bb[1]), "=&v"(bb[2]), "=&v"(bb[3]) : "v"(bvo), "s"(b1p) : "memory");
    });
    CWAIT(7, 4);
    compute(6, [&](int i) __attribute__((always_inline)) {
      if (i == 0) dma4k(Kh + (size_t)tb0 * SLOT, vlin, lds0 + (grp ? 6 : 9) * SLOT + wq * 4096);
      if (i == 1) dma4k(Vh + (size_t)tb0 * SLOT, vlin, lds0 + (grp ? 7 : 0) * SLOT + wq * 4096);
      if (i == 2) dma4k(Kh + (size_t)(tb0 + 1) * SLOT, vlin, lds0 + (grp ? 8 : 1) * SLOT + wq * 4096);
    });
    compute(7, [&](int) __attribute__((always_inline)) {});
    STAMP(1, 2);
    asm volatile("s_waitcnt vmcnt(8) lgkmcnt(0)\n\ts_barrier" : "+v"(bb[0]), "+v"(bb[1]), "+v"(bb[2]), "+v"(bb[3]) :: "memory"); SCHED;
#undef XLOAD
#undef CWAIT
#undef WFRAG
#pragma unroll
    for (int u = 0; u < 4; ++u) { qf[u][0] = pack8(aq[2 * u][0], aq[2 * u + 1][0], QSCALE); qf[u][1] = pack8(aq[2 * u][1], aq[2 * u + 1][1], QSCALE); }
    if (grp == 1) {
      LAS char* st = lds + 4 * SLOT + wq * 8192 + lane * 16;
#pragma unroll
      for (int i = 0; i < 8; ++i) *(LAS bf16x8*)(st + i * 1024) = qf[i >> 1][i & 1];
    }
    if constexpr (DOF) {
      const int sbase = (blk0 * NKT + 64 + head) * 16384;
#pragma unroll
      for (int v = 0; v < 2; ++v)
#pragma unroll
        for (int qt = 0; qt < 2; ++qt) {
          u32x4 w;
#pragma unroll
          for (int hf = 0; hf < 2; ++hf) {
            const f32x4 a = aq[8 + 2 * v + hf][qt]; const u32x2 bv = bb[2 * v + hf];
            float t0 = rbf(rbf(a[0]) + bflo(bv[0])), t1 = rbf(rbf(a[1]) + bfhi(bv[0])), t2 = rbf(rbf(a[2]) + bflo(bv[1])), t3 = rbf(rbf(a[3]) + bfhi(bv[1]));
            t0 = t0 > 0.f ? t0 : 0.f; t1 = t1 > 0.f ? t1 : 0.f; t2 = t2 > 0.f ? t2 : 0.f; t3 = t3 > 0.f ? t3 : 0.f;
            w[2 * hf] = cvtpk(t0 * t0, t1 * t1); w[2 * hf + 1] = cvtpk(t2 * t2, t3 * t3);
          }
          __builtin_amdgcn_raw_buffer_store_b128(w, arsrc, cvoff + sbase + v * 8192 + qt * 256, 0, 0);
        }
    }
  };
  if (doF) prologue(std::true_type{}); else prologue(std::false_type{});
  {
    const __amdgpu_buffer_rsrc_t frs = __builtin_amdgcn_make_buffer_rsrc((void*)(slabs + (size_t)b * 6144), (short)0, 24576, 0x00020000);
#pragma unroll
    for (int i = 0; i < 3; ++i) __builtin_amdgcn_raw_buffer_store_b128((u32x4){SENT, SENT, SENT, SENT}, frs, tid * 16 + i * 8192, 0, 17);
  }
  const int klane = g * 1024 + c * 16, vlane = g * 2048 + c * 16;
  const int kslot0 = (grp ? 6 : 9) * SLOT, kslot1 = (grp ? 8 : 1) * SLOT, vslot0 = (grp ? 7 : 0) * SLOT, vslot1 = (grp ? 3 : 2) * SLOT;
  f32x4 o[8][2], sc[4][2], negm[2];
#pragma unroll
  for (int i = 0; i < 8; ++i) { o[i][0] = (f32x4){0.f, 0.f, 0.f, 0.f}; o[i][1] = (f32x4){0.f, 0.f, 0.f, 0.f}; }
  negm[0] = (f32x4){0.f, 0.f, 0.f, 0.f}; negm[1] = (f32x4){0.f, 0.f, 0.f, 0.f};
  bf16x8 pf[2][2];
  float mref[2] = {0.f, 0.f};
  f32x4 lacc[2] = {(f32x4){0.f, 0.f, 0.f, 0.f}, (f32x4){0.f, 0.f, 0.f, 0.f}};
  bf16x8 ones = {0x3f80, 0x3f80, 0x3f80, 0x3f80, 0x3f80, 0x3f80, 0x3f80, 0x3f80};
  asm volatile("" : "+v"(ones));
  auto store_ctx = [&](int blk, float inv0, float inv1) __attribute__((always_inline)) {
    const int sbase = (blk * NKT + 2 * head) * 16384;
#pragma unroll
    for (int v = 0; v < 4; ++v)
#pragma unroll
      for (int qt = 0; qt < 2; ++qt)
        __builtin_amdgcn_raw_buffer_store_b128(__builtin_bit_cast(u32x4, pack8(o[2 * v][qt], o[2 * v + 1][qt], qt ? inv1 : inv0)), arsrc, cvoff + sbase + v * 8192 + qt * 256, 0, 0);
  };
  constexpr int PD = 6;
  bf16x8 fr_[32];
  const int swk = grp ? 99 : sw;
  int ktile = tb0 + 1, vtile = tb0;
  unsigned kvo = 0, vvo = 0;
#define FRD(i) (((i) < 16) ? *(LAS const bf16x8*)(kp + ((i) >> 2) * 4096 + ((i) & 3) * 256) : *(LAS const bf16x8*)(vp + (((i) - 16) >> 3) * 8192 + (((i) - 16) & 7) * 256))
#define SETM0(v) asm volatile("s_mov_b32 m0, %0\n\ts_nop 0" :: "s"(v) : "memory")
#define DMAI(vo, base, off) asm volatile("global_load_lds_dwordx4 %0, %1 offset:" #off :: "v"(vo), "s"(base) : "memory")
#define MBODY(i) do { if (i < 16) { sc[i & 3][0] = MFMA16(fr_[i], qf[i >> 2][0], i < 4 ? negm[0] : sc[i & 3][0]); sc[i & 3][1] = MFMA16(fr_[i], qf[i >> 2][1], i < 4 ? negm[1] : sc[i & 3][1]); } \
      else { o[(i - 16) & 7][0] = MFMA16(fr_[i], pf[(i - 16) >> 3][0], o[(i - 16) & 7][0]); o[(i - 16) & 7][1] = MFMA16(fr_[i], pf[(i - 16) >> 3][1], o[(i - 16) & 7][1]); \
        if (((i - 16) & 7) == 3) lacc[0] = MFMA16(ones, pf[(i - 16) >> 3][0], lacc[0]); if (((i - 16) & 7) == 5) lacc[1] = MFMA16(ones, pf[(i - 16) >> 3][1], lacc[1]); } } while (0)
#define KPIECE(s_, j) do { if ((j) == 0) { ktile = ((s_) + 2 == swk) ? 0 : ktile + 1; kvo = vlin + (unsigned)min(ktile, 31) * SLOT; SETM0(lds0 + (((s_) & 1) ? kslot1 : kslot0) + wq * 4096); DMAI(kvo, Kh, 0); } \
    if ((j) == 1) DMAI(kvo, Kh, 1024); if ((j) == 2) DMAI(kvo, Kh, 2048); if ((j) == 3) DMAI(kvo, Kh, 3072); } while (0)
#define VPIECE(s_, j) do { if ((j) == 0) { SETM0(lds0 + (((s_) & 1) ? vslot1 : vslot0) + wq * 4096); DMAI(vvo, Vh, 0); } \
    if ((j) == 1) DMAI(vvo, Vh, 1024); if ((j) == 2) DMAI(vvo, Vh, 2048); if ((j) == 3) DMAI(vvo, Vh, 3072); } while (0)
#define MPRE(s_, I0) do { \
    LAS const char* kp = lds + (((s_) & 1) ? kslot1 : kslot0) + klane; \
    LAS const char* vp = lds + (((s_) & 1) ? vslot0 : vslot1) + vlane; \
    _Pragma("unroll") for (int i = I0; i < I0 + PD; ++i) fr_[i] = FRD(i); \
    (void)kp; (void)vp; } while (0)
#define MSLOT(s_, I0, I1, DM) do { \
    LAS const char* kp = lds + (((s_) & 1) ? kslot1 : kslot0) + klane; \
    LAS const char* vp = lds + (((s_) & 1) ? vslot0 : vslot1) + vlane; \
    if (DM) { vtile = ((s_) == swk) ? 0 : vtile + 1; vvo = vlin + (unsigned)min(vtile, 31) * SLOT; } \
    _Pragma("unroll") for (int i = I0; i < I1; ++i) { \
      if (i + PD < I1) fr_[i + PD] = FRD(i + PD); \
      MBODY(i); \
      if ((DM) && (i & 3) == 2 && i < 16) VPIECE(s_, i >> 2); \
      SCHED; \
    } (void)kp; (void)vp; } while (0)
#define SCV(i) sc[((i) >> 2) & 3][(i) >> 4][(i) & 3]
  auto sslot = [&](const int s, const bool SLOW) __attribute__((always_inline)) {
    KPIECE(s, 0);
    bool first = false;
    if (__builtin_expect(SLOW, 0)) {
      const bool swst = (s == swk);
      if (swst) {
        store_ctx(x, 1.0f / lacc[0][0], 1.0f / lacc[1][0]);
#pragma unroll
        for (int i = 0; i < 8; ++i) { o[i][0] = (f32x4){0.f, 0.f, 0.f, 0.f}; o[i][1] = (f32x4){0.f, 0.f, 0.f, 0.f}; }
        lacc[0] = (f32x4){0.f, 0.f, 0.f, 0.f}; lacc[1] = (f32x4){0.f, 0.f, 0.f, 0.f};
      }
      first = (s == 0) || swst;
      const int jb = grp ? s - 15 : s - (sw - 2);
      if (jb >= 0 && jb <= 1) {
        const int dq = 32 * wq + c - 64 * jb - 4 * g;
#pragma unroll
        for (int i = 0; i < 32; ++i) if (16 * ((i >> 2) & 3) + (i & 3) > dq + 16 * (i >> 4)) SCV(i) = -__builtin_inff();
      }
    }
    float pm0 = max3f(SCV(0), SCV(1), SCV(2)), pm1 = max3f(SCV(16), SCV(17), SCV(18));
#pragma unroll
    for (int i = 3; i < 15; i += 2) { pm0 = max3f(pm0, SCV(i), SCV(i + 1)); pm1 = max3f(pm1, SCV(16 + i), SCV(17 + i)); }
    pm0 = max2f(pm0, SCV(15)); pm1 = max2f(pm1, SCV(31));
    KPIECE(s, 1);
    if (__builtin_expect(first || __any(max2f(pm0, pm1) > THR), 0)) {
      const float r0 = xg_max(pm0), r1 = xg_max(pm1);
      const float dl[2] = {first ? r0 : fmaxf(r0, 0.f), first ? r1 : fmaxf(r1, 0.f)};
#pragma unroll
      for (int qt = 0; qt < 2; ++qt) {
        mref[qt] += dl[qt];
#pragma unroll
        for (int kt = 0; kt < 4; ++kt) sc[kt][qt] -= dl[qt];
        if (!first) {
          const float al = __builtin_amdgcn_exp2f(-dl[qt]);
          lacc[qt] *= al;
#pragma unroll
          for (int d = 0; d < 8; ++d) o[d][qt] *= al;
        }
        negm[qt] = (f32x4){-mref[qt], -mref[qt], -mref[qt], -mref[qt]};
      }
    }
    {
      unsigned pw[16];
      SCV(0) = __builtin_amdgcn_exp2f(SCV(0)); SCV(1) = __builtin_amdgcn_exp2f(SCV(1)); SCHED;
#pragma unroll
      for (int i = 0; i < 32; i += 2) {
        if (i + 2 < 32) SCV(i + 2) = __builtin_amdgcn_exp2f(SCV(i + 2));
        pw[i >> 1] = cvtpk(SCV(i), SCV(i + 1));
        if (i + 3 < 32) SCV(i + 3) = __builtin_amdgcn_exp2f(SCV(i + 3));
        if (i == 8) KPIECE(s, 2);
        if (i == 20) KPIECE(s, 3);
        SCHED;
      }
#pragma unroll
      for (int k4 = 0; k4 < 4; ++k4) pf[k4 & 1][k4 >> 1] = __builtin_bit_cast(bf16x8, (u32x4){pw[4 * k4], pw[4 * k4 + 1], pw[4 * k4 + 2], pw[4 * k4 + 3]});
    }
    if (__builtin_expect(SLOW, 0)) {
      if (s + 1 == swk) {
        LAS const char* st = lds + 4 * SLOT + wq * 8192 + lane * 16;
#pragma unroll
        for (int i = 0; i < 8; ++i) qf[i >> 1][i & 1] = *(LAS const bf16x8*)(st + i * 1024);
        mref[0] = 0.f; mref[1] = 0.f; negm[0] = (f32x4){0.f, 0.f, 0.f, 0.f}; negm[1] = (f32x4){0.f, 0.f, 0.f, 0.f};
      }
    }
    asm volatile("" : "+v"(pf[0][0]), "+v"(pf[0][1]), "+v"(pf[1][0]), "+v"(pf[1][1]), "+v"(negm[0]), "+v"(negm[1]));
  };
  auto warm_k3 = [&]() __attribute__((always_inline)) {
#pragma unroll
    for (int i = 0; i < 3; ++i) {
      int sec = (wq * 3 + i) * 64 + lane; sec = sec < 736 ? sec : 735;
      const int kr = 1472 * ((b & 7) >> 1) + 46 * (b >> 3) + (sec >> 4);
      const char* src = (kr < 4096 ? (const char*)Wo + (size_t)kr * 1024 : (const char*)W2 + (size_t)(kr - 4096) * 1024) + (sec & 15) * 64;
      touch64(src, lds0 + 8 * SLOT + wq * 256);
    }
  };
  const unsigned spec = grp ? 0x18001u : (0x0001u | (7u << (sw - 2)));
  LAS char* pb = lds + (wq == 0 ? 9 : (wq == 1 ? 1 : 2 + wq)) * SLOT + lane * 16;
  LAS char* mlp = lds + 2 * SLOT + wq * 1024 + lane * 16;
  STAMP(1, 3);
  if (grp == 1) LBAR();
  MPRE(0, 0); SCHED;
  MSLOT(0, 0, 16, false);
  WBAR(0);
  for (int s = 0; s < 16; ++s) {
    LSTAMP(5);
    sslot(s, (spec >> s) & 1u);
    MPRE(s + 1, 0);
    LSTAMP(6);
    if (__builtin_expect(s == swk, 0)) VBAR(12); else VBAR(4);
    LSTAMP(7);
    MSLOT(s + 1, 0, 32, true);
    LSTAMP(8);
    WBAR(4);
  }
  STAMP(1, 37);
  sslot(16, true);
  WBAR(0);
  if (grp == 1) warm_k3();
  MPRE(17, 16); SCHED;
  MSLOT(17, 16, 32, false);
  STAMP(1, 40);
  if (grp == 0) {
#pragma unroll
    for (int i = 0; i < 16; ++i) *(LAS f32x4*)(pb + i * 1024) = o[i >> 1][i & 1];
    *(LAS f32x4*)mlp = (f32x4){mref[0], lacc[0][0], mref[1], lacc[1][0]};
    LBAR();
  } else {
    const f32x4 ml = *(LAS const f32x4*)mlp;
    float ga[2], gb[2];
#pragma unroll
    for (int qt = 0; qt < 2; ++qt) {
      const float mn = fmaxf(ml[2 * qt], mref[qt]), fa = __builtin_amdgcn_exp2f(ml[2 * qt] - mn), fb = __builtin_amdgcn_exp2f(mref[qt] - mn);
      const float inv = 1.0f / (lacc[qt][0] * fb + ml[2 * qt + 1] * fa);
      ga[qt] = fa * inv; gb[qt] = fb * inv;
    }
#pragma unroll
    for (int i = 0; i < 16; ++i) { const f32x4 pa = *(LAS const f32x4*)(pb + i * 1024); o[i >> 1][i & 1] = o[i >> 1][i & 1] * gb[i & 1] + pa * ga[i & 1]; }
    store_ctx(15 - x, 1.0f, 1.0f);
  }
#undef FRD
#undef MSLOT
#undef MPRE
#undef MBODY
#undef KPIECE
#undef VPIECE
#undef SCV
  STAMP(1, 41); STAMPR(1, 63);
}

constexpr int K3_NS = 5, K3_LDS = K3_NS * 32768;
__global__ __launch_bounds__(512, 2) void k3_out(const u16* __restrict__ Abuf, const u16* __restrict__ Wo, const u16* __restrict__ W2, const u16* __restrict__ x,
                                                 const u16* __restrict__ b2, unsigned* __restrict__ slabs, u16* __restrict__ out) {
  extern __shared__ __attribute__((aligned(16))) char smem[];
  LAS char* lds = (LAS char*)smem;
  const unsigned lds0 = (unsigned)(uintptr_t)lds;
  const int tid = threadIdx.x, lane = tid & 63, wid = rfl(tid >> 6), kh = wid >> 2, wr = (wid >> 1) & 1, wc = wid & 1, fr = lane & 15, fq = lane >> 4;
  const int b = blockIdx.x, xcd = b & 7, idx = b >> 3, sp = xcd >> 1, rt = (xcd & 1) * 8 + (idx >> 2), ct = idx & 3;
  const int kt0 = sp * 23;
  const char* Ab = (const char*)Abuf + (size_t)(rt * NKT + kt0) * 16384;
  const unsigned voffa = (unsigned)(wid * 2048 + lane * 16);
  const unsigned voffb = (unsigned)((32 * (wid >> 2) + 16 * (lane >> 5) + 4 * (wid & 3) + ((lane >> 3) & 3)) * DM + 128 * ct + 16 * (((lane >> 1) & 3) ^ (((lane >> 4) & 1) | ((wid & 1) << 1))) + 8 * (lane & 1)) * 2u;
  auto stage = [&](int it) __attribute__((always_inline)) {
    const int kt = kt0 + it;
    const char* wb = kt < 64 ? (const char*)Wo + (size_t)kt * 64 * DM * 2 : (const char*)W2 + (size_t)(kt - 64) * 64 * DM * 2;
    const unsigned d = lds0 + (it % K3_NS) * 32768 + wid * 2048;
    dma2k(Ab + (size_t)it * 16384, voffa, d);
    dma1k(wb, voffb, d + 16384); dma1k(wb + 128, voffb, d + 16384 + 1024);
  };
  const int aoff = (4 * kh + fq) * 2048 + (wr * 64 + fr) * 16;
  int boff[4];
#pragma unroll
  for (int n = 0; n < 4; ++n) boff[n] = 16384 + (4 * kh + fq) * 2048 + wc * 1024 + ((lane >> 2) & 3) * 128 + ((n ^ (((lane >> 3) & 1) | ((fq & 1) << 1))) * 32) + 8 * (lane & 3);
  f32x4 acc[4][4];
#pragma unroll
  for (int m = 0; m < 4; ++m)
#pragma unroll
    for (int n = 0; n < 4; ++n) acc[m][n] = (f32x4){0.f, 0.f, 0.f, 0.f};
  bf16x8 a0[4], b0[4], a1[4], b1[4];
#define K3_RDB(bf, it_, n) bf[n] = vtr2(lds + ((it_) % K3_NS) * 32768 + boff[n], lds + ((it_) % K3_NS) * 32768 + boff[n] + 512)
#define K3_RDA(af, it_, m) af[m] = *(LAS const bf16x8*)(lds + ((it_) % K3_NS) * 32768 + aoff + (m) * 256)
#define K3_STEP(it_, ac, bc, an, bn, RD, ST) do { \
    if ((ST) && kh == 0) stage((it_) + 5); \
    _Pragma("unroll") for (int m = 0; m < 4; ++m) { \
      if (RD) { K3_RDB(bn, (it_) + 1, m); K3_RDA(an, (it_) + 1, m); } \
      _Pragma("unroll") for (int n = 0; n < 4; ++n) acc[m][n] = MFMA16(bc[n], ac[m], acc[m][n]); \
      SCHED; \
      if (m == 1 && (ST) && kh == 1) { stage((it_) + 5); SCHED; } } } while (0)
  STAMPR(2, 62); STAMP(2, 0);
  stage(0); stage(1); stage(2); stage(3); stage(4);
  WBAR(16);
#pragma unroll
  for (int n = 0; n < 4; ++n) { K3_RDB(b0, 0, n); K3_RDA(a0, 0, n); }
  STAMP(2, 1);
#pragma unroll
  for (int j = 0; j < 18; j += 2) {
    WBAR(12); K3_STEP(j, a0, b0, a1, b1, true, true);
    WBAR(12); K3_STEP(j + 1, a1, b1, a0, b0, true, true);
  }
  WBAR(12); K3_STEP(18, a0, b0, a1, b1, true, false);
  WBAR(8); K3_STEP(19, a1, b1, a0, b0, true, false);
  WBAR(4); K3_STEP(20, a0, b0, a1, b1, true, false);
  WBAR(0); K3_STEP(21, a1, b1, a0, b0, true, false);
  K3_STEP(22, a0, b0, a1, b1, false, false);
#undef K3_RDA
#undef K3_RDB
#undef K3_STEP
  f32x4 r2[2][4];
  {
    LAS char* mine = lds + wid * 8192 + lane * 16; LAS const char* theirs = lds + (wid ^ 4) * 8192 + lane * 16;
#pragma unroll
    for (int mm = 0; mm < 2; ++mm)
#pragma unroll
      for (int n = 0; n < 4; ++n) *(LAS f32x4*)(mine + (mm * 4 + n) * 1024) = kh ? acc[mm][n] : acc[2 + mm][n];
    LBAR();
#pragma unroll
    for (int mm = 0; mm < 2; ++mm)
#pragma unroll
      for (int n = 0; n < 4; ++n) { const f32x4 t = *(LAS const f32x4*)(theirs + (mm * 4 + n) * 1024); r2[mm][n] = kh ? t + acc[2 + mm][n] : acc[mm][n] + t; }
  }
  STAMP(2, 30);
  const int tile = rt * 4 + ct;
  const __amdgpu_buffer_rsrc_t rs = __builtin_amdgcn_make_buffer_rsrc((void*)(slabs + (size_t)tile * 12 * 2048), (short)0, 12 * 8192, 0x00020000);
  const int sl = wid * 1024 + lane * 16;
  f32x4 own[2] = {r2[0][0], r2[1][0]};
#pragma unroll
  for (int q = 0; q < 4; ++q) {
    if (q != sp) {
      u32x4 w = __builtin_bit_cast(u32x4, pack8(r2[0][q], r2[1][q], 1.0f));
#pragma unroll
      for (int e = 0; e < 4; ++e) w[e] = w[e] == SENT ? 0x7fc07fc0u : w[e];
      __builtin_amdgcn_raw_buffer_store_b128(w, rs, (q * 3 + (sp > q ? sp - 1 : sp)) * 8192 + sl, 0, 17);
    } else { own[0] = r2[0][q]; own[1] = r2[1][q]; }
  }
  STAMP(2, 31); STAMPR(2, 63);
  {
    const int col = ct * 128 + 64 * wc + 16 * sp + 4 * fq;
    const size_t roff = (size_t)(rt * 128 + 64 * wr + 32 * kh + fr) * DM + col;
    const u32x2 bb = *(const u32x2*)(b2 + col);
    u32x2 xx[2];
#pragma unroll
    for (int mm = 0; mm < 2; ++mm) xx[mm] = *(const u32x2*)(x + roff + (size_t)mm * 16 * DM);
    u32x4 t[3];
    for (int tries = 0; tries < (1 << 14); ++tries) {
      bool ok = true;
      asm volatile("" ::: "memory");
#pragma unroll
      for (int o = 0; o < 3; ++o) t[o] = __builtin_amdgcn_raw_buffer_load_b128(rs, (sp * 3 + o) * 8192 + sl, 0, 17);
#pragma unroll
      for (int o = 0; o < 3; ++o)
#pragma unroll
        for (int e = 0; e < 4; ++e) ok = ok && (t[o][e] != SENT);
      if (__all(ok)) break;
      __builtin_amdgcn_s_sleep(4);
    }
#pragma unroll
    for (int mm = 0; mm < 2; ++mm) {
      f32x4 tv[3];
#pragma unroll
      for (int o = 0; o < 3; ++o) tv[o] = (f32x4){bflo(t[o][2 * mm]), bfhi(t[o][2 * mm]), bflo(t[o][2 * mm + 1]), bfhi(t[o][2 * mm + 1])};
      const f32x4 p0 = sp == 0 ? own[mm] : tv[0], p1 = sp == 1 ? own[mm] : (sp < 1 ? tv[0] : tv[1]), p2 = sp == 2 ? own[mm] : (sp < 2 ? tv[1] : tv[2]), p3 = sp == 3 ? own[mm] : tv[2];
      const f32x4 a = ((p0 + p1) + p2) + p3;
      u32x2 w = {cvtpk(a[0] + bflo(xx[mm][0]) + bflo(bb[0]), a[1] + bfhi(xx[mm][0]) + bfhi(bb[0])), cvtpk(a[2] + bflo(xx[mm][1]) + bflo(bb[1]), a[3] + bfhi(xx[mm][1]) + bfhi(bb[1]))};
      *(u32x2*)(out + roff + (size_t)mm * 16 * DM) = w;
    }
  }
  STAMP(2, 32); STAMPR(2, 61);
}

extern "C" void kernel_launch(void* const* d_in, const int* in_sizes, int n_in, void* d_out, int out_size, void* d_ws, size_t ws_size,
                              hipStream_t stream) {
  const u16* x = (const u16*)d_in[0];
  const u16* ckv = (const u16*)d_in[1];
  const u16* wq = (const u16*)d_in[2];
  const u16* wuk = (const u16*)d_in[3];
  const u16* wuv = (const u16*)d_in[4];
  const u16* wo = (const u16*)d_in[5];
  const u16* w1 = (const u16*)d_in[7];
  const u16* b1 = (const u16*)d_in[8];
  const u16* w2 = (const u16*)d_in[9];
  const u16* b2 = (const u16*)d_in[10];
  char* ws = (char*)d_ws;
  u16* Kws = (u16*)(ws + OFF_K); u16* Vws = (u16*)(ws + OFF_V); u16* xn_t = (u16*)(ws + OFF_XN); u16* Abuf = (u16*)(ws + OFF_A);
  unsigned* slabs = (unsigned*)(ws + OFF_SLAB);
  (void)hipFuncSetAttribute((const void*)k1_kv, hipFuncAttributeMaxDynamicSharedMemorySize, K1_LDS + 256);
  (void)hipFuncSetAttribute((const void*)k2_attn, hipFuncAttributeMaxDynamicSharedMemorySize, K2_LDS);
  (void)hipFuncSetAttribute((const void*)k3_out, hipFuncAttributeMaxDynamicSharedMemorySize, K3_LDS);
  k1_kv<<<256, 512, K1_LDS + 256, stream>>>(x, ckv, wuk, wuv, Kws, Vws, xn_t, wq, w1, wo, w2);
  k2_attn<<<256, 512, K2_LDS, stream>>>(xn_t, wq, w1, b1, Kws, Vws, Abuf, slabs, wo, w2);
  k3_out<<<256, 512, K3_LDS, stream>>>(Abuf, wo, w2, x, b2, slabs, (u16*)d_out);

}
```
